```python
import torch

def _softmax(x, axis=-1):
    return torch.softmax(x, dim=axis)
def _gelu(x):
    return torch.nn.functional.gelu(x, approximate="tanh")
def _silu(x):
    return torch.nn.functional.silu(x)
def _sigmoid(x):
    return torch.sigmoid(x)
def _layernorm(x, axis=-1, eps=1e-5):
    m = x.mean(dim=axis, keepdim=True)
    v = x.var(dim=axis, keepdim=True, unbiased=False)
    return (x - m) / torch.sqrt(v + eps)
def _scatter_add(tgt, idx, src, axis=0):
    return tgt.clone().index_add_(axis, idx, src)
def _segment_sum(src, seg, n):
    out = torch.zeros((n,) + src.shape[1:], dtype=src.dtype)
    return out.index_add_(0, seg, src)
def _pad(x, pw):
    flat = tuple(v for pair in reversed(pw) for v in pair)
    return torch.nn.functional.pad(x, flat)

kernel_name = 'concat_heads__sliding_local_dilated__softmax_full_mha__prompt'

def setup_inputs():

    _w = (lambda a: (lambda t: t.to(torch.bfloat16) if t.is_floating_point() else t)(torch.as_tensor(a)))
    def _A_setup():
        B, S, D = 1, 2048, 448
        return {
            "x": _w((lambda *s: torch.randn(*s, dtype=torch.bfloat16) * 0.4)(B, S, D)),
            "wqkv": _w((lambda *s: torch.randn(*s, dtype=torch.bfloat16) * 0.8)(D, 3 * D)),
            "wo": _w((lambda *s: torch.randn(*s, dtype=torch.bfloat16) * 0.1)(D, D)),
        }
    _A_in = _A_setup()

    def _B_setup():
        B, S, D = 1, 2048, 448
        return {
            "x": _w((lambda *s: torch.randn(*s, dtype=torch.bfloat16) * 0.2)(B, S, D)),
            "wqkv": _w((lambda *s: torch.randn(*s, dtype=torch.bfloat16) * 0.8)(D, 3 * D)),
            "wo": _w((lambda *s: torch.randn(*s, dtype=torch.bfloat16) * 0.2)(D, D)),
        }
    _B_in = _B_setup()

    return {
        "A_x": _A_in["x"],
        "A_wqkv": _A_in["wqkv"],
        "A_wo": _A_in["wo"],
        "B_x": _B_in["x"],
        "B_wqkv": _B_in["wqkv"],
        "B_wo": _B_in["wo"],
    }

def reference(A_x, A_wqkv, A_wo, B_x, B_wqkv, B_wo):
    def _A_ref(x, wqkv, wo):
        B, S, D = x.shape
        H = 16; Dh = D // H
        W, dil = 8, 2
        qkv = (lambda x, *d: x.permute(*d))((x @ wqkv).reshape(B, S, 3, H, Dh), 2, 0, 3, 1, 4)
        q, k, v = qkv[0], qkv[1], qkv[2]
        scale = Dh ** -0.5
        pos = torch.arange(S)
        off = torch.arange(W) * dil
        idx = pos[:, None] - off[None, :]
        mask = idx >= 0
        idx_c = torch.where(mask, idx, 0).reshape(-1)
        kg = k[:, :, idx_c, :].reshape(B, H, S, W, Dh)
        vg = v[:, :, idx_c, :].reshape(B, H, S, W, Dh)
        logits = torch.einsum("bhsd,bhswd->bhsw", q, kg) * scale
        logits = torch.where(mask[None, None, :, :], logits, -1e9)
        attn = _softmax(logits, axis=-1)
        o = torch.einsum("bhsw,bhswd->bhsd", attn, vg)
        return (lambda x, *d: x.permute(*d))(o, 0, 2, 1, 3).reshape(B, S, D) @ wo

    def _B_ref(x, wqkv, wo):
        B, S, D = x.shape
        H = 16; Dh = D // H
        qkv = (lambda x, *d: x.permute(*d))((x @ wqkv).reshape(B, S, 3, H, Dh), 2, 0, 3, 1, 4)
        q, k, v = qkv[0], qkv[1], qkv[2]
        scale = Dh ** -0.5
        scores = torch.einsum("bhqd,bhkd->bhqk", q, k) * scale
        p = _softmax(scores, axis=-1)
        o = torch.einsum("bhqk,bhkd->bhqd", p, v)
        return (lambda x, *d: x.permute(*d))(o, 0, 2, 1, 3).reshape(B, S, D) @ wo

    a = _A_ref(A_x, A_wqkv, A_wo)
    b = _B_ref(B_x, B_wqkv, B_wo)
    return torch.concatenate([a, b], axis=-1)
```

```cpp
#include <hip/hip_runtime.h>
#include <hip/hip_bf16.h>
#include <stdint.h>

typedef short bf16x8 __attribute__((ext_vector_type(8)));
typedef short s16x4 __attribute__((ext_vector_type(4)));
typedef float f32x16 __attribute__((ext_vector_type(16)));
typedef unsigned u32x4 __attribute__((ext_vector_type(4)));
typedef unsigned u32x2 __attribute__((ext_vector_type(2)));
#define LAS __attribute__((address_space(3)))
#define GAS __attribute__((address_space(1)))
typedef LAS const char* lds_cptr;
typedef LAS char* lds_ptr;

constexpr int S = 2048, D = 448, H = 16, DH = 28, N3 = 1344;
constexpr int NT = S / 32;
constexpr size_t WS_Q = 0;
constexpr size_t WS_KV = 4u << 20;
constexpr size_t WS_O = 12u << 20;
constexpr float QSCALE = 0.18898223650461362f * 1.4426950408889634f;

#define MFMA32(a, b, c) __builtin_amdgcn_mfma_f32_32x32x16_bf16(a, b, c, 0, 0, 0)
#define WAIT_V(n) asm volatile("s_waitcnt vmcnt(" #n ")" ::: "memory")
#define BAR() do { asm volatile("s_waitcnt lgkmcnt(0)" ::: "memory"); __builtin_amdgcn_s_barrier(); } while (0)

typedef __bf16 bf16x2_t __attribute__((ext_vector_type(2)));
typedef float f32x2 __attribute__((ext_vector_type(2)));
__device__ __forceinline__ unsigned cvtpk(float lo, float hi) { return __builtin_bit_cast(unsigned, __builtin_convertvector((f32x2){lo, hi}, bf16x2_t)); }
__device__ __forceinline__ void glds16(const void* g, lds_ptr l) { __builtin_amdgcn_global_load_lds((const GAS unsigned*)g, (LAS unsigned*)l, 16, 0, 0); }
__device__ __forceinline__ s16x4 trread(lds_cptr p) { return __builtin_bit_cast(s16x4, __builtin_amdgcn_ds_read_tr16_b64_v4i16((LAS s16x4*)p)); }
__device__ __forceinline__ bf16x8 ldsfrag(lds_cptr p) { return *(const LAS bf16x8*)p; }
__device__ __forceinline__ bf16x8 cat8(s16x4 a, s16x4 b) { return (bf16x8){a[0], a[1], a[2], a[3], b[0], b[1], b[2], b[3]}; }
__device__ __forceinline__ u32x4 pack8(const f32x16& a, int s) {
    return (u32x4){cvtpk(a[8 * s], a[8 * s + 1]), cvtpk(a[8 * s + 2], a[8 * s + 3]), cvtpk(a[8 * s + 4], a[8 * s + 5]), cvtpk(a[8 * s + 6], a[8 * s + 7])};
}
__device__ __forceinline__ float swap32_other(float x, int hh) {
    auto r = __builtin_amdgcn_permlane32_swap(__float_as_uint(x), __float_as_uint(x), false, false);
    return __uint_as_float(hh ? r[0] : r[1]);
}

constexpr int K1_SLOT = 16384 + 12288;
__global__ __launch_bounds__(256, 2) void k_qkv(const char* __restrict__ xA, const char* __restrict__ wA, const char* __restrict__ xB, const char* __restrict__ wB, char* __restrict__ ws) {
    __shared__ __attribute__((aligned(16))) char smem[2 * K1_SLOT + 64];
    const int tid = threadIdx.x, lane = tid & 63, w = __builtin_amdgcn_readfirstlane(tid >> 6);
    const int b = blockIdx.x, br = b >> 8, xcd = b & 7, loc = (b >> 3) & 31;
    const int rt = (xcd & 3) * 4 + (loc & 3), h = (xcd >> 2) * 8 + (loc >> 2);
    const char* x = br ? xB : xA; const char* W = br ? wB : wA;
    lds_ptr L = (lds_ptr)smem;
    int xo[4], wo[3];
#pragma unroll
    for (int j = 0; j < 4; ++j) { const int i = 4 * w + j, row = 8 * i + (lane >> 3), c = (lane & 7) ^ ((row >> 1) & 7); xo[j] = (rt * 128 + row) * 896 + c * 16; }
#pragma unroll
    for (int j = 0; j < 3; ++j) { const int p = 64 * (3 * w + j) + lane, row = p / 12, q = p % 12; wo[j] = row * 2688 + ((56 * (16 * (q >> 2) + h)) & ~15) + 16 * (q & 3); }
    auto stage = [&](int c, int slot) {
#pragma unroll
        for (int j = 0; j < 4; ++j) glds16(x + xo[j] + c * 128, L + slot * K1_SLOT + (4 * w + j) * 1024);
#pragma unroll
        for (int j = 0; j < 3; ++j) glds16(W + wo[j] + c * (64 * 2688), L + slot * K1_SLOT + 16384 + (3 * w + j) * 1024);
    };
    const int r = lane & 31, hh = lane >> 5;
    const int xrow = 32 * w + r;
    const int xbase = xrow * 128, xsw = (xrow >> 1) & 7;
    const int wbase = 16384 + (8 * hh + ((lane & 15) >> 2)) * 192 + 8 * (h & 1) + ((lane >> 4) & 1) * 32 + 8 * (lane & 3);
    f32x16 aq = {}, ak = {}, av = {};
    stage(0, 0);
    for (int c = 0; c < 7; ++c) {
        WAIT_V(0); BAR();
        if (c + 1 < 7) stage(c + 1, (c + 1) & 1);
        lds_cptr sl = (lds_cptr)L + (c & 1) * K1_SLOT;
#pragma unroll
        for (int ks = 0; ks < 4; ++ks) {
            const bf16x8 xf = ldsfrag(sl + xbase + (((2 * ks + hh) ^ xsw) << 4));
            lds_cptr wp = sl + wbase + ks * 16 * 192;
            const bf16x8 wq = cat8(trread(wp), trread(wp + 4 * 192));
            const bf16x8 wk = cat8(trread(wp + 64), trread(wp + 64 + 4 * 192));
            const bf16x8 wv = cat8(trread(wp + 128), trread(wp + 128 + 4 * 192));
            aq = MFMA32(wq, xf, aq); ak = MFMA32(wk, xf, ak); av = MFMA32(xf, wv, av);
        }
    }
#pragma unroll
    for (int i = 0; i < 16; ++i) aq[i] *= QSCALE;
    if (hh) { aq[12] = aq[13] = aq[14] = aq[15] = 0.f; ak[12] = ak[13] = ak[14] = ak[15] = 0.f; }
    if (r >= 28) { const float f = (r == 28) ? 1.f : 0.f;
#pragma unroll
        for (int i = 0; i < 16; ++i) av[i] = f; }
    const int tt = rt * 4 + w;
    char* qd = ws + WS_Q + ((size_t)((br * 16 + h) * 64 + tt) << 11) + lane * 16;
    char* kd = ws + WS_KV + ((size_t)((br * 16 + h) * 64 + tt) << 12) + lane * 16;
    *(u32x4*)(qd) = pack8(aq, 0); *(u32x4*)(qd + 1024) = pack8(aq, 1);
    *(u32x4*)(kd) = pack8(ak, 0); *(u32x4*)(kd + 1024) = pack8(ak, 1);
    *(u32x4*)(kd + 2048) = pack8(av, 0); *(u32x4*)(kd + 3072) = pack8(av, 1);
}

__device__ __forceinline__ float colmax16(const f32x16& s) {
    float a = fmaxf(fmaxf(s[0], s[1]), s[2]), b = fmaxf(fmaxf(s[3], s[4]), s[5]);
    a = fmaxf(fmaxf(a, s[6]), s[7]); b = fmaxf(fmaxf(b, s[8]), s[9]);
    a = fmaxf(fmaxf(a, s[10]), s[11]); b = fmaxf(fmaxf(b, s[12]), s[13]);
    a = fmaxf(fmaxf(a, s[14]), s[15]);
    return fmaxf(a, b);
}
__device__ __forceinline__ void store_o(char* ws, int br, int tt, int h, int lane, int hh, f32x16& o) {
    const float lo = swap32_other(o[12], hh);
    const float l = hh ? o[12] : lo;
    const float rl = 1.f / l;
#pragma unroll
    for (int i = 0; i < 16; ++i) o[i] *= rl;
    if (hh) { o[12] = o[13] = o[14] = o[15] = 0.f; }
    char* od = ws + WS_O + ((size_t)((br * 64 + tt) * 16 + h) << 11) + lane * 16;
    *(u32x4*)(od) = pack8(o, 0); *(u32x4*)(od + 1024) = pack8(o, 1);
}

constexpr float M_BIAS = 40.f;
constexpr int K2_SLOT = 32768;
__global__ __launch_bounds__(512, 2) void k_attn(char* __restrict__ ws) {
    __shared__ __attribute__((aligned(16))) char smem[3 * K2_SLOT];
    const int tid = threadIdx.x, lane = tid & 63, w = __builtin_amdgcn_readfirstlane(tid >> 6);
    const int r = lane & 31, hh = lane >> 5;
    const int b = blockIdx.x;
    lds_ptr L = (lds_ptr)smem;
    if (b >= 256) {
        const int item = (b - 256) * 8 + w, h = item >> 6, tt = item & 63;
        const char* qs = ws + WS_Q + ((size_t)(h * 64 + tt) << 11) + lane * 16;
        const char* kc = ws + WS_KV + ((size_t)(h * 64 + tt) << 12) + lane * 16;
        const char* kp = tt ? kc - 4096 : kc;
        const bf16x8 q0 = *(const bf16x8*)qs, q1 = *(const bf16x8*)(qs + 1024);
        f32x16 sc = {}, sp = {};
        sc = MFMA32(*(const bf16x8*)kc, q0, sc); sc = MFMA32(*(const bf16x8*)(kc + 1024), q1, sc);
        sp = MFMA32(*(const bf16x8*)kp, q0, sp); sp = MFMA32(*(const bf16x8*)(kp + 1024), q1, sp);
        float m = -INFINITY;
#pragma unroll
        for (int i = 0; i < 16; ++i) {
            const int key = (i & 3) + 8 * (i >> 2) + 4 * hh;
            const int dc = r - key, dp = r + 32 - key;
            const bool vc = (dc >= 0) && (dc <= 14) && !(dc & 1);
            const bool vp = (dp <= 14) && !(dp & 1) && (tt > 0);
            sc[i] = vc ? sc[i] : -INFINITY; sp[i] = vp ? sp[i] : -INFINITY;
            m = fmaxf(m, fmaxf(sc[i], sp[i]));
        }
        m = fmaxf(m, swap32_other(m, hh));
#pragma unroll
        for (int i = 0; i < 16; ++i) { sc[i] = __builtin_amdgcn_exp2f(sc[i] - m); sp[i] = __builtin_amdgcn_exp2f(sp[i] - m); }
        f32x16 o = {};
        o = MFMA32(*(const bf16x8*)(kc + 2048), __builtin_bit_cast(bf16x8, pack8(sc, 0)), o);
        o = MFMA32(*(const bf16x8*)(kc + 3072), __builtin_bit_cast(bf16x8, pack8(sc, 1)), o);
        o = MFMA32(*(const bf16x8*)(kp + 2048), __builtin_bit_cast(bf16x8, pack8(sp, 0)), o);
        o = MFMA32(*(const bf16x8*)(kp + 3072), __builtin_bit_cast(bf16x8, pack8(sp, 1)), o);
        store_o(ws, 0, tt, h, lane, hh, o);
        return;
    }
    const int xcd = b & 7, loc = b >> 3;
    const int h = xcd * 2 + (loc >> 4), rt = loc & 15;
    const int tt = rt * 4 + (w & 3), g = w >> 2;
    const char* kv = ws + WS_KV + ((size_t)((16 + h) * 64) << 12);
    auto stage = [&](int c, int slot) {
#pragma unroll
        for (int j = 0; j < 4; ++j) glds16(kv + c * K2_SLOT + (4 * w + j) * 1024 + lane * 16, L + slot * K2_SLOT + (4 * w + j) * 1024);
    };
    const char* qs = ws + WS_Q + ((size_t)((16 + h) * 64 + tt) << 11) + lane * 16;
    const bf16x8 q0 = *(const bf16x8*)qs, q1 = *(const bf16x8*)(qs + 1024);
    asm volatile("" :: "v"(q0), "v"(q1));
    stage(0, 0); stage(1, 1);
    f32x16 o = {}, negm = {};
    float m = 0.f;
    int slot = 0;
    for (int c = 0; c < 8; ++c) {
        if (c < 7) WAIT_V(4); else WAIT_V(0);
        BAR();
        if (c + 2 < 8) stage(c + 2, slot == 0 ? 2 : slot - 1);
        lds_cptr sl = (lds_cptr)L + slot * K2_SLOT + g * 16384 + lane * 16;
        if (c == 0) {
            f32x16 s = {};
            s = MFMA32(ldsfrag(sl), q0, s); s = MFMA32(ldsfrag(sl + 1024), q1, s);
            m = colmax16(s); m = fmaxf(m, swap32_other(m, hh)) + M_BIAS;
#pragma unroll
            for (int i = 0; i < 16; ++i) negm[i] = -m;
        }
#pragma unroll
        for (int t = 0; t < 4; ++t) {
            lds_cptr tp = sl + t * 4096;
            f32x16 s = MFMA32(ldsfrag(tp), q0, negm); s = MFMA32(ldsfrag(tp + 1024), q1, s);
#pragma unroll
            for (int i = 0; i < 16; ++i) s[i] = __builtin_amdgcn_exp2f(s[i]);
            o = MFMA32(ldsfrag(tp + 2048), __builtin_bit_cast(bf16x8, pack8(s, 0)), o);
            o = MFMA32(ldsfrag(tp + 3072), __builtin_bit_cast(bf16x8, pack8(s, 1)), o);
        }
        slot = slot == 2 ? 0 : slot + 1;
    }
    {
        float t = 0.f;
#pragma unroll
        for (int i = 0; i < 16; ++i) t += fabsf(o[i]) * 0x1p-8f;
        if (__builtin_expect(__any(!(t < 0x1p110f)), 0)) {
            o = f32x16{}; m = -INFINITY;
            for (int c = 0; c < 8; ++c)
                for (int t4 = 0; t4 < 4; ++t4) {
                    const char* tp = kv + (size_t)(c * 8 + g * 4 + t4) * 4096 + lane * 16;
                    f32x16 s = {};
                    s = MFMA32(*(const bf16x8*)tp, q0, s); s = MFMA32(*(const bf16x8*)(tp + 1024), q1, s);
                    float mx = colmax16(s); mx = fmaxf(mx, swap32_other(mx, hh));
                    const float mn = fmaxf(m, mx), al = __builtin_amdgcn_exp2f(m - mn);
                    m = mn;
#pragma unroll
                    for (int i = 0; i < 16; ++i) { s[i] = __builtin_amdgcn_exp2f(s[i] - mn); o[i] *= al; }
                    o = MFMA32(*(const bf16x8*)(tp + 2048), __builtin_bit_cast(bf16x8, pack8(s, 0)), o);
                    o = MFMA32(*(const bf16x8*)(tp + 3072), __builtin_bit_cast(bf16x8, pack8(s, 1)), o);
                }
        }
    }
    BAR();
    LAS float* ex = (LAS float*)L + (w & 3) * (17 * 64) + lane;
    if (g == 1) {
#pragma unroll
        for (int i = 0; i < 16; ++i) ex[i * 64] = o[i];
        ex[16 * 64] = m;
    }
    BAR();
    if (g == 0) {
        const float m2 = ex[16 * 64], mm = fmaxf(m, m2), f1 = __builtin_amdgcn_exp2f(m - mm), f2 = __builtin_amdgcn_exp2f(m2 - mm);
#pragma unroll
        for (int i = 0; i < 16; ++i) o[i] = o[i] * f1 + ex[i * 64] * f2;
        store_o(ws, 1, tt, h, lane, hh, o);
    }
}

__global__ __launch_bounds__(512, 2) void k_oproj(const char* __restrict__ woA, const char* __restrict__ woB, const char* __restrict__ ws, char* __restrict__ out) {
    __shared__ __attribute__((aligned(16))) char smem[452 * 256];
    const int tid = threadIdx.x, lane = tid & 63, w = __builtin_amdgcn_readfirstlane(tid >> 6);
    const int r = lane & 31, hh = lane >> 5;
    const int b = blockIdx.x, br = b >> 8, tt = (b & 255) >> 2, cg = b & 3;
    const char* wo = br ? woB : woA;
    lds_ptr L = (lds_ptr)smem;
    {
        const int q = lane >> 4, cp = (lane & 15) ^ (q << 2);
        const int cb = min(cg * 256 + cp * 16, 896 - 16);
        for (int i = w; i < 113; i += 8) { const int row = min(4 * i, 444) + q; glds16(wo + row * 896 + cb, L + i * 1024); }
    }
    const int nt = w & 3, kh = w >> 2;
    const char* of = ws + WS_O + ((size_t)((br * 64 + tt) * 16 + kh * 8) << 11) + lane * 16;
    bf16x8 ofr[16];
#pragma unroll
    for (int i = 0; i < 16; ++i) ofr[i] = *(const bf16x8*)(of + i * 1024);
    WAIT_V(0); BAR();
    f32x16 acc = {};
    {
        const int q = (lane & 15) >> 2, p = lane & 3, cb = (lane >> 4) & 1;
        const int c = 4 * nt + 2 * cb + (p >> 1);
        lds_cptr wp = (lds_cptr)L + (kh * 8 * 28 + 4 * hh + q) * 256 + ((c ^ (q << 2)) << 4) + 8 * (p & 1);
#pragma unroll
        for (int i = 0; i < 16; ++i) {
            lds_cptr a = wp + ((i >> 1) * 28 + (i & 1) * 16) * 256;
            acc = MFMA32(cat8(trread(a), trread(a + 8 * 256)), ofr[i], acc);
        }
    }
    BAR();
    LAS float* ex = (LAS float*)L + nt * (16 * 64) + lane;
    if (kh == 1) {
#pragma unroll
        for (int i = 0; i < 16; ++i) ex[i * 64] = acc[i];
    }
    BAR();
    if (kh == 0 && cg * 4 + nt < 14) {
#pragma unroll
        for (int i = 0; i < 16; ++i) acc[i] += ex[i * 64];
        char* od = out + (size_t)(tt * 32 + r) * 1792 + br * 896 + (cg * 4 + nt) * 64 + hh * 8;
#pragma unroll
        for (int g4 = 0; g4 < 4; ++g4) *(u32x2*)(od + g4 * 16) = (u32x2){cvtpk(acc[4 * g4], acc[4 * g4 + 1]), cvtpk(acc[4 * g4 + 2], acc[4 * g4 + 3])};
    }
}

extern "C" void kernel_launch(void* const* d_in, const int* in_sizes, int n_in, void* d_out, int out_size, void* d_ws, size_t ws_size, hipStream_t stream) {
    const char* xA = (const char*)d_in[0]; const char* wA = (const char*)d_in[1]; const char* woA = (const char*)d_in[2];
    const char* xB = (const char*)d_in[3]; const char* wB = (const char*)d_in[4]; const char* woB = (const char*)d_in[5];
    char* ws = (char*)d_ws;
    k_qkv<<<512, 256, 0, stream>>>(xA, wA, xB, wB, ws);
    k_attn<<<256 + 128, 512, 0, stream>>>(ws);
    k_oproj<<<512, 512, 0, stream>>>(woA, woB, ws, (char*)d_out);
}
```

```cpp
#include <hip/hip_runtime.h>
#include <hip/hip_bf16.h>
#include <stdint.h>
#include <type_traits>

#define STAMP(i)
#define STAMP_DECL
#define STAMP_FLUSH()

typedef short bf16x8 __attribute__((ext_vector_type(8)));
typedef short s16x4 __attribute__((ext_vector_type(4)));
typedef float f32x16 __attribute__((ext_vector_type(16)));
typedef float f32x4 __attribute__((ext_vector_type(4)));
typedef float f32x2 __attribute__((ext_vector_type(2)));
typedef unsigned u32x4 __attribute__((ext_vector_type(4)));
typedef unsigned u32x2 __attribute__((ext_vector_type(2)));
typedef __bf16 bf16x2_t __attribute__((ext_vector_type(2)));
typedef unsigned long long u64;
#define LAS __attribute__((address_space(3)))
#define GAS __attribute__((address_space(1)))
typedef LAS const char* lds_cptr;
typedef LAS char* lds_ptr;

constexpr unsigned WS_FP = 1u << 16;
constexpr unsigned WS_FOA = 2u << 16, WS_FOB = 3u << 16;
constexpr unsigned WS_Q = 1u << 20;
constexpr unsigned WS_KV = 5u << 20;
constexpr unsigned WS_OA = 13u << 20, WS_OB = 15u << 20;
constexpr unsigned WS_BYTES = 17u << 20;
constexpr int P1_SLOT = 32768, P1_W = 8192;
constexpr int L_WOS = 0, L_FLG = 49152, L_CNT = 50176, L_KV = 65536, L_CMB = 65536, L_OB = 98304;
constexpr int L_OA = 131072, L_QEX = 131072, L_LKV = 131072, L_LQ = 131072 + 20480, LDS_BYTES = 163840;
#define IDLE_SLEEP 8
constexpr float LOG2E = 1.4426950408889634f;
constexpr float QSCALE = 0.18898223650461362f * LOG2E;
constexpr float M_BIAS = 40.f;

#define MFMA32(a, b, c) __builtin_amdgcn_mfma_f32_32x32x16_bf16(a, b, c, 0, 0, 0)
#define WAIT_V(n) asm volatile("s_waitcnt vmcnt(%0)" :: "i"(n) : "memory")
#define BAR() asm volatile("s_waitcnt lgkmcnt(0)\n\ts_barrier" ::: "memory")
#define BAR_RAW() asm volatile("s_barrier" ::: "memory")
#define SBAR() __builtin_amdgcn_sched_barrier(0)
#define PIN(x) asm volatile("" : "+v"(x))
#define RLX_AGENT __ATOMIC_RELAXED, __HIP_MEMORY_SCOPE_AGENT
#define EX2(x) __builtin_amdgcn_exp2f(x)

__device__ __forceinline__ unsigned cvtpk(float lo, float hi) { return __builtin_bit_cast(unsigned, __builtin_convertvector((f32x2){lo, hi}, bf16x2_t)); }
template <int OFF> __device__ __forceinline__ void dma16(const char* base, unsigned voff, unsigned lds) {
    asm volatile("s_add_u32 m0, %2, %3\n\ts_nop 0\n\tglobal_load_lds_dwordx4 %0, %1" :: "v"(voff), "s"(base), "s"(lds), "i"(OFF) : "memory", "scc"); }
template <int OFF, int IO> __device__ __forceinline__ void dma16_dev(const char* base, unsigned voff, unsigned lds) {
    asm volatile("s_add_u32 m0, %2, %3\n\ts_nop 0\n\tglobal_load_lds_dwordx4 %0, %1 offset:%4 sc1" :: "v"(voff), "s"(base), "s"(lds), "i"(OFF), "i"(IO) : "memory", "scc"); }
template <int OFF> __device__ __forceinline__ void dma16_dev2(const char* base, unsigned voff, unsigned lds) {
    asm volatile("s_add_u32 m0, %2, %3\n\ts_nop 0\n\tglobal_load_lds_dwordx4 %0, %1 sc1\n\tglobal_load_lds_dwordx4 %0, %1 offset:1024 sc1" :: "v"(voff), "s"(base), "s"(lds), "i"(OFF) : "memory", "scc"); }
template <int OFF> __device__ __forceinline__ void dma16_dev4(const char* base, unsigned voff, unsigned lds) {
    asm volatile("s_add_u32 m0, %2, %3\n\ts_nop 0\n\tglobal_load_lds_dwordx4 %0, %1 sc1\n\tglobal_load_lds_dwordx4 %0, %1 offset:1024 sc1\n\t"
                 "global_load_lds_dwordx4 %0, %1 offset:2048 sc1\n\tglobal_load_lds_dwordx4 %0, %1 offset:3072 sc1" :: "v"(voff), "s"(base), "s"(lds), "i"(OFF) : "memory", "scc"); }
__device__ __forceinline__ void dma16_x8(const char* base, const unsigned (&v)[8], unsigned lds, unsigned ls) {
    asm volatile("s_mov_b32 m0, %9\n\ts_nop 0\n\tglobal_load_lds_dwordx4 %0, %8\n\t"
                 "s_add_u32 m0, m0, %10\n\ts_nop 0\n\tglobal_load_lds_dwordx4 %1, %8\n\t" "s_add_u32 m0, m0, %10\n\ts_nop 0\n\tglobal_load_lds_dwordx4 %2, %8\n\t"
                 "s_add_u32 m0, m0, %10\n\ts_nop 0\n\tglobal_load_lds_dwordx4 %3, %8\n\t" "s_add_u32 m0, m0, %10\n\ts_nop 0\n\tglobal_load_lds_dwordx4 %4, %8\n\t"
                 "s_add_u32 m0, m0, %10\n\ts_nop 0\n\tglobal_load_lds_dwordx4 %5, %8\n\t" "s_add_u32 m0, m0, %10\n\ts_nop 0\n\tglobal_load_lds_dwordx4 %6, %8\n\t"
                 "s_add_u32 m0, m0, %10\n\ts_nop 0\n\tglobal_load_lds_dwordx4 %7, %8"
                 :: "v"(v[0]), "v"(v[1]), "v"(v[2]), "v"(v[3]), "v"(v[4]), "v"(v[5]), "v"(v[6]), "v"(v[7]), "s"(base), "s"(lds), "s"(ls) : "memory", "scc"); }
__device__ __forceinline__ s16x4 trread(lds_cptr p) { return __builtin_bit_cast(s16x4, __builtin_amdgcn_ds_read_tr16_b64_v4i16((LAS s16x4*)p)); }
__device__ __forceinline__ bf16x8 ldsfrag(lds_cptr p) { return *(const LAS bf16x8*)p; }
__device__ __forceinline__ bf16x8 cat8(s16x4 a, s16x4 b) { return (bf16x8){a[0], a[1], a[2], a[3], b[0], b[1], b[2], b[3]}; }
__device__ __forceinline__ u32x4 pack8(const f32x16& a, int s) {
    return (u32x4){cvtpk(a[8 * s], a[8 * s + 1]), cvtpk(a[8 * s + 2], a[8 * s + 3]), cvtpk(a[8 * s + 4], a[8 * s + 5]), cvtpk(a[8 * s + 6], a[8 * s + 7])};
}
__device__ __forceinline__ bf16x8 asfrag(u32x4 v) { return __builtin_bit_cast(bf16x8, v); }
__device__ __forceinline__ float max3f(float a, float b, float c) { float d; asm("v_max3_f32 %0, %1, %2, %3" : "=v"(d) : "v"(a), "v"(b), "v"(c)); return d; }
__device__ __forceinline__ float halves_max(float x) {
    auto r = __builtin_amdgcn_permlane32_swap(__float_as_uint(x), __float_as_uint(x), false, false);
    return fmaxf(__uint_as_float(r[0]), __uint_as_float(r[1]));
}
__device__ __forceinline__ float colmax16(const f32x16& s) {
    float a = fmaxf(fmaxf(s[0], s[1]), s[2]), b = fmaxf(fmaxf(s[3], s[4]), s[5]);
    a = fmaxf(fmaxf(a, s[6]), s[7]); b = fmaxf(fmaxf(b, s[8]), s[9]);
    a = fmaxf(fmaxf(a, s[10]), s[11]); b = fmaxf(fmaxf(b, s[12]), s[13]);
    a = fmaxf(fmaxf(a, s[14]), s[15]);
    return fmaxf(a, b);
}
__device__ __forceinline__ float denom(const f32x16& o, bool odd) {
    const float own = odd ? o[0] : o[12];
    auto r = __builtin_amdgcn_permlane32_swap(__float_as_uint(own), __float_as_uint(own), false, false);
    return __uint_as_float(odd ? r[0] : r[1]);
}
__device__ __forceinline__ void wait_flag(const char* f, u64 tag) {
#pragma nounroll
    for (unsigned s = 0; s < (1u << 22); ++s) {
        if (__all(__hip_atomic_load((GAS u64*)f, RLX_AGENT) == tag)) break;
        __builtin_amdgcn_s_sleep(2);
    }
    asm volatile("" ::: "memory");
}
__device__ __forceinline__ void wait_flag3(const char* f0, const char* f1, const char* f2, u64 tag) {
#pragma nounroll
    for (unsigned s = 0; s < (1u << 22); ++s) {
        const u64 a = __hip_atomic_load((GAS u64*)f0, RLX_AGENT), b = __hip_atomic_load((GAS u64*)f1, RLX_AGENT), c = __hip_atomic_load((GAS u64*)f2, RLX_AGENT);
        if (__all(a == tag && b == tag && c == tag)) break;
        __builtin_amdgcn_s_sleep(4);
    }
    asm volatile("" ::: "memory");
}
__device__ __forceinline__ void set_flag(char* f, u64 tag) { __hip_atomic_store((GAS u64*)f, tag, RLX_AGENT); }

__device__ __forceinline__ unsigned tile_addr(int ct, int k, int br, int tt, int r, int hh) {
    const int seg = ct >= 28 ? 2 : (ct >= 14 ? 1 : 0);
    const unsigned base = seg == 0 ? WS_Q + (br << 21) + (tt << 11) : WS_KV + (br << 22) + (tt << 12) + (seg - 1) * 2048;
    const int G = (ct - 14 * seg) * 4 + 2 * k + hh, hp = (G * 37) >> 8, gi = G - 7 * hp;
    const int p = gi > 3 ? gi - 3 : gi;
    return base + ((2 * hp + (gi > 3)) << (seg == 0 ? 17 : 18)) + p * 512 + ((r ^ (seg == 2 ? 4 * p : 0)) << 4) + (gi == 3);
}
__device__ __forceinline__ void store_tile(const f32x16& a, int ct, unsigned ad0, unsigned ad1, __amdgpu_buffer_rsrc_t wsr) {
    const float sc = ct < 14 ? QSCALE : 1.f;
    const unsigned pad = ct >= 28 ? 0x3f80u : 0u;
#pragma unroll
    for (int k = 0; k < 2; ++k) {
        auto r0 = __builtin_amdgcn_permlane32_swap(cvtpk(a[8 * k] * sc, a[8 * k + 1] * sc), cvtpk(a[8 * k + 4] * sc, a[8 * k + 5] * sc), false, false);
        auto r1 = __builtin_amdgcn_permlane32_swap(cvtpk(a[8 * k + 2] * sc, a[8 * k + 3] * sc), cvtpk(a[8 * k + 6] * sc, a[8 * k + 7] * sc), false, false);
        u32x4 v = (u32x4){r0[0], r1[0], r0[1], r1[1]};
        unsigned ad = k ? ad1 : ad0;
        if (ad & 1) {
            --ad;
            __builtin_amdgcn_raw_buffer_store_b128((u32x4){pad, 0u, v[2], v[3]}, wsr, (ad ^ (ct >= 28 ? 192 : 0)) - 1536 + (ct < 14 ? 1 << 17 : 1 << 18), 0, 16);
            v[2] = pad; v[3] = 0u;
        }
        __builtin_amdgcn_raw_buffer_store_b128(v, wsr, ad, 0, 16);
    }
}

__global__ __launch_bounds__(512, 2) void fused_kernel(const char* __restrict__ xA, const char* __restrict__ wA, const char* __restrict__ woA,
                                                       const char* __restrict__ xB, const char* __restrict__ wB, const char* __restrict__ woB,
                                                       char* ws, char* __restrict__ out) {
    __shared__ __attribute__((aligned(16))) char smem[LDS_BYTES];
    const lds_ptr L = (lds_ptr)smem;
    const unsigned L0 = (unsigned)(uintptr_t)L;
    const int tid = threadIdx.x, lane = tid & 63, w = __builtin_amdgcn_readfirstlane(tid >> 6);
    const int g = w >> 2, j = w & 3, r = lane & 31, hh = lane >> 5;
    const int b = blockIdx.x, xcd = b & 7, loc = b >> 3;
    const int rt = loc & 15, h = 2 * xcd + (loc >> 4);
    const int pr = xcd * 4 + (loc & 3), pc = loc >> 2;
    STAMP_DECL;
    STAMP(0);
    const __amdgpu_buffer_rsrc_t wsr = __builtin_amdgcn_make_buffer_rsrc(ws, 0, WS_BYTES, 0x00020000);
    u64 tag;
#define LOAD_TAG() do { asm volatile("s_load_dwordx2 %0, %1, 0x0\n\ts_waitcnt lgkmcnt(0)" : "=s"(tag) : "s"(ws) : "memory"); tag += 1; } while (0)
    const int fcol = 28 * h + 27 * hh;
    auto kv_wait = [&]() {
        if (w == 0) {
            const char* f = ws + WS_FP + 16384 + r * 64;
            wait_flag3(f + (fcol / 192) * 2048, f + ((448 + fcol) / 192) * 2048, f + ((896 + fcol) / 192) * 2048, tag);
            STAMP(3);
        }
    };
    auto kv_prefetch = [&]() {
        const char* s = ws + WS_KV + (1u << 22) + ((h * 64 + j) << 12);
        dma16_dev2<L_QEX>(ws + WS_Q + (1u << 21) + ((h * 64 + rt * 4 + j) << 11), lane * 16, L0 + j * 2048);
        dma16_dev4<L_KV>(s + (rt << 14), lane * 16, L0 + j * 4096);
        dma16_dev4<L_KV + 16384>(s + (((rt + 1) & 15) << 14), lane * 16, L0 + j * 4096);
        dma16_dev4<L_KV + 32768>(s + (((rt + 2) & 15) << 14), lane * 16, L0 + j * 4096);
    };

    if (loc < 28) {
        unsigned vo[8];
        {
            const unsigned p = 64 * (j - 1) + lane, prow = (p * 2731) >> 16, pcn = p - 24 * prow;
            const unsigned st2 = j == 0 ? 16 * 896 : 16 * 2688;
            vo[0] = j == 0 ? (lane >> 3) * 896 + ((lane & 7) ^ (lane >> 4)) * 16 : prow * 2688 + (pcn ^ ((prow & 2) << 1)) * 16;
            vo[1] = j == 0 ? (vo[0] ^ 64) + 8 * 896 : vo[0] + 8 * 2688;
#pragma unroll
            for (int i = 2; i < 8; ++i) vo[i] = vo[i - 2] + st2;
        }
        const int start = (pc + 2 * (loc & 3)) % 7;
        const int roleoff = j == 0 ? pr * (64 * 896) : pc * 384;
        const int kstride = j == 0 ? 128 : 64 * 2688;
        const unsigned lrole = L0 + (j == 0 ? 0 : P1_W + (j - 1) * 1024), lstep = j == 0 ? 1024 : 3072;
        const char* srcB = (j == 0 ? xB : wB) + roleoff;
        const char* srcA = (j == 0 ? xA : wA) + roleoff;
        int islot = 0, ikc = start;
        auto issue = [&](const char* src) {
            dma16_x8(src + ikc * kstride, vo, lrole + islot * P1_SLOT, lstep);
            islot = (islot + 1) & 3; ikc = ikc == 6 ? 0 : ikc + 1;
        };
        if (g == 1) { issue(srcB); issue(srcB); }
        LOAD_TAG();
        const int xrow = 32 * (j & 1) + r;
        const int xb = xrow * 128 + ((hh ^ ((xrow >> 1) & 7)) << 4);
        const int tq = (lane & 15) >> 2;
        const int wrow = P1_W + (8 * hh + tq) * 384 + ((lane >> 4) & 1) * 32 + 8 * (lane & 3);
        const int wb0 = wrow + (((3 * (j >> 1)) ^ (tq >> 1)) << 6), wb1 = wrow + (((3 * (j >> 1) + 1) ^ (tq >> 1)) << 6), wb2 = wrow + (((3 * (j >> 1) + 2) ^ (tq >> 1)) << 6);
        const int ct0 = pc * 6 + 3 * (j >> 1), ptt = pr * 2 + (j & 1);
        unsigned sad[6];
#pragma unroll
        for (int i = 0; i < 6; ++i) { sad[i] = tile_addr(ct0 + (i >> 1), i & 1, 1 - g, ptt, r, hh); PIN(sad[i]); }
        f32x16 a0 = {}, a1 = {}, a2 = {};
        bf16x8 xf[4], w0[4], w1[4], w2[4];
        xf[2] = xf[3] = w0[2] = w0[3] = w1[2] = w1[3] = w2[2] = w2[3] = bf16x8{};
        PIN(a0); PIN(a1); PIN(a2); PIN(xf[2]); PIN(xf[3]); PIN(w0[2]); PIN(w0[3]); PIN(w1[2]); PIN(w1[3]); PIN(w2[2]); PIN(w2[3]);
#define P1_STEP(km, kr) do { \
            a0 = MFMA32(w0[km], xf[km], a0); SBAR(); \
            xf[kr] = ldsfrag(sl + (xb ^ ((kr) << 5))); w0[kr] = cat8(trread(sl + wb0 + (kr) * 6144), trread(sl + wb0 + (kr) * 6144 + 1536)); SBAR(); \
            a1 = MFMA32(w1[km], xf[km], a1); SBAR(); \
            w1[kr] = cat8(trread(sl + wb1 + (kr) * 6144), trread(sl + wb1 + (kr) * 6144 + 1536)); SBAR(); \
            a2 = MFMA32(w2[km], xf[km], a2); SBAR(); \
            w2[kr] = cat8(trread(sl + wb2 + (kr) * 6144), trread(sl + wb2 + (kr) * 6144 + 1536)); SBAR(); } while (0)
#define P1_MMA(ks) do { a0 = MFMA32(w0[ks], xf[ks], a0); a1 = MFMA32(w1[ks], xf[ks], a1); a2 = MFMA32(w2[ks], xf[ks], a2); SBAR(); } while (0)
        if (g == 1) {
#pragma nounroll
            for (int c = 0; c < 7; ++c) { WAIT_V(8); BAR_RAW(); issue(c < 5 ? srcB : srcA); }
            WAIT_V(8);
        }
#pragma nounroll
        for (int c = 7 * g; c < 7 * g + 7; ++c) {
            BAR_RAW();
            STAMP(16 + c - 7 * g);
            const lds_cptr sl = (lds_cptr)L + (c & 3) * P1_SLOT;
            P1_STEP(2, 0); P1_STEP(3, 1); P1_STEP(0, 2); P1_STEP(1, 3);
            WAIT_V(0);
        }
        if (g == 1) {
            STAMP(4);
            P1_MMA(2); P1_MMA(3);
            store_tile(a0, ct0, sad[0], sad[1], wsr); store_tile(a1, ct0 + 1, sad[2], sad[3], wsr); store_tile(a2, ct0 + 2, sad[4], sad[5], wsr);
        } else {
            islot = 1; ikc = start + 2 >= 7 ? start - 5 : start + 2;
            BAR_RAW(); STAMP(2);
            issue(srcA);
            P1_MMA(2); P1_MMA(3);
            store_tile(a0, ct0, sad[0], sad[1], wsr);
            BAR_RAW();
            issue(srcA);
            store_tile(a1, ct0 + 1, sad[2], sad[3], wsr); store_tile(a2, ct0 + 2, sad[4], sad[5], wsr);
            STAMP(12);
            WAIT_V(12);
#pragma nounroll
            for (int c = 9; c < 12; ++c) {
                BAR_RAW();
                if (c == 10 && tid == 64) set_flag(ws + WS_FP + 16384 + (pc * 32 + pr) * 64, tag);
                if (c == 10) STAMP(13);
                issue(srcA);
                WAIT_V(8);
            }
            BAR_RAW();
            STAMP(14);
            WAIT_V(0); kv_wait();
            BAR_RAW();
            kv_prefetch();
            STAMP(4);
        }
    } else {
        LOAD_TAG();
#pragma nounroll
        for (int i = 0; i < IDLE_SLEEP; ++i) __builtin_amdgcn_s_sleep(16);
        kv_wait();
        BAR_RAW();
        if (g == 0) kv_prefetch();
    }
    STAMP(5);
    const int tto = rt * 4 + (h & 3), cg = h >> 2;
    const bool odd = h & 1;
    bf16x8 wf[32];
    {
        f32x16 o = {};
        float m;
        bf16x8 q0, q1;
        const lds_cptr kvp = (lds_cptr)L + L_KV + g * 8192 + lane * 16;
        const unsigned wso = (8 * j + (lane >> 4)) * 896 + min((unsigned)(cg * 256 + (((lane & 15) ^ ((lane >> 4) << 2)) << 4)), 880u), wso1 = wso + 4 * 896;
        const unsigned lw2 = L0 + w * 2048, lane16 = lane * 16;
        const char* wsrc = g ? woB : woA;
        const char* kvsrc = ws + WS_KV + (1u << 22) + (h << 18) + w * 2048;
        const int tq = (lane & 15) >> 2, tp = lane & 3;
        const lds_cptr wrp = (lds_cptr)L + L_WOS + g * 8192 + (4 * hh + tq) * 256 + (((4 * j + 2 * ((lane >> 4) & 1) + (tp >> 1)) ^ (tq << 2)) << 4) + 8 * (tp & 1);
        const int vpc = 2 * ((lane >> 4) & 1) + (tp >> 1);
        const int vo0 = 2048 + vpc * 512 + (((4 * hh + tq) ^ (vpc << 2)) << 4) + 8 * (tp & 1), vo1 = vo0 ^ 128;
        const lds_cptr vp0 = (lds_cptr)L + L_KV + g * 8192 + vo0, vp1 = (lds_cptr)L + L_KV + g * 8192 + vo1;
        f32x16 sA, sB, negm;
        bf16x8 kf0, kf1, vf0, vf1;
        u32x4 pA, pB;
        f32x4 et;
#define TILE_OFF(n) ((((n) >> 1) & 3) * 16384 + ((n) & 1) * 4096)
#define EXP4(S, i) do { S[i] = EX2(S[i]); S[i + 1] = EX2(S[i + 1]); S[i + 2] = EX2(S[i + 2]); S[i + 3] = EX2(S[i + 3]); } while (0)
#define STEP(n, SC, SN, X1, X2, X3) do { \
            if ((n) < 31) SN = MFMA32(kf0, q0, negm); \
            X1; SBAR(); \
            if ((n) > 0) { pB[2] = cvtpk(et[0], et[1]); pB[3] = cvtpk(et[2], et[3]); PIN(pB); SBAR(); } \
            EXP4(SC, 0); PIN(SC); SBAR(); \
            if ((n) > 0) o = MFMA32(vf1, asfrag(pB), o); \
            vf1 = cat8(trread(vp0 + TILE_OFF(n) + 256), trread(vp1 + TILE_OFF(n) + 256)); X2; SBAR(); \
            pA[0] = cvtpk(SC[0], SC[1]); pA[1] = cvtpk(SC[2], SC[3]); PIN(pA); SBAR(); \
            EXP4(SC, 4); PIN(SC); SBAR(); \
            if ((n) < 31) SN = MFMA32(kf1, q1, SN); \
            if ((n) < 30) { kf0 = ldsfrag(kvp + TILE_OFF((n) + 2)); kf1 = ldsfrag(kvp + TILE_OFF((n) + 2) + 1024); } \
            SBAR(); \
            pA[2] = cvtpk(SC[4], SC[5]); pA[3] = cvtpk(SC[6], SC[7]); PIN(pA); SBAR(); \
            EXP4(SC, 8); PIN(SC); SBAR(); \
            o = MFMA32(vf0, asfrag(pA), o); \
            if ((n) < 31) vf0 = cat8(trread(vp0 + TILE_OFF((n) + 1)), trread(vp1 + TILE_OFF((n) + 1))); \
            X3; SBAR(); \
            pB[0] = cvtpk(SC[8], SC[9]); pB[1] = cvtpk(SC[10], SC[11]); PIN(pB); SBAR(); \
            et[0] = EX2(SC[12]); et[1] = EX2(SC[13]); et[2] = EX2(SC[14]); et[3] = EX2(SC[15]); PIN(et); SBAR(); } while (0)
#define WO_READ(p, s) do { if ((p) >= 0) wf[2 * (p) + (s)] = cat8(trread(wrp + ((p) % 3) * 16384 + (s) * 4096), trread(wrp + ((p) % 3) * 16384 + (s) * 4096 + 2048)); } while (0)
#define KV_DMA(it, u) do { \
            if ((it) < 13) dma16_dev<L_KV + (((it) + 3) & 3) * 16384, (u) * 1024>(kvsrc + (((rt + (it) + 3) & 15) << 14), lane16, lw2); \
            else if ((it) < 15) dma16_dev<L_OA + ((it) - 13) * 16384, (u) * 1024>(ws + WS_OA + (tto << 15) + ((it) - 13) * 16384 + w * 2048, lane16, lw2); } while (0)
#define WO_DMA(it, u) dma16<L_WOS + ((it) % 3) * 16384 + (u) * 1024>(wsrc + (28 * (it) - 4 * ((it) & 1)) * 896, (u) ? wso1 : wso, lw2)
#define ITER(it) do { \
            if ((it) == 5) WAIT_V(8); else WAIT_V(4);             \
            if ((it) == 0) BAR(); else asm volatile("s_waitcnt lgkmcnt(4)\n\ts_barrier" ::: "memory"); \
            STAMP(32 + (it)); \
            side(std::integral_constant<int, (it)>{}); \
            SBAR(); \
            STEP(2 * (it), sA, sB, WO_READ((it) - 2, 0), KV_DMA(it, 0), WO_DMA(it, 0)); \
            STEP(2 * (it) + 1, sB, sA, WO_READ((it) - 2, 1), KV_DMA(it, 1), WO_DMA(it, 1)); } while (0)
        auto side = [&](auto itc) {
            constexpr int it = decltype(itc)::value;
            if constexpr (it == 0) {
                if (tid == 0) *(LAS unsigned*)(L + L_CNT) = 0u;
                q0 = ldsfrag((lds_cptr)L + L_QEX + j * 2048 + lane * 16); q1 = ldsfrag((lds_cptr)L + L_QEX + j * 2048 + 1024 + lane * 16);
                sA = f32x16{};
                sA = MFMA32(ldsfrag(kvp), q0, sA); sA = MFMA32(ldsfrag(kvp + 1024), q1, sA);
                kf0 = ldsfrag(kvp + TILE_OFF(1)); kf1 = ldsfrag(kvp + TILE_OFF(1) + 1024); vf0 = cat8(trread(vp0), trread(vp1));
                m = halves_max(colmax16(sA)) + M_BIAS;
#pragma unroll
                for (int i = 0; i < 16; ++i) { negm[i] = -m; sA[i] -= m; }
                PIN(negm);
            }
            if constexpr (it == 1) { if (tid == 256 && loc < 28) set_flag(ws + WS_FP + (pc * 32 + pr) * 64, tag); }
            if constexpr (it == 3 || it == 4) {
                const unsigned afl = ((((lane >> 2) & 3) % 3 * 448 + fcol) / 192 * 32 + max(2 * rt - 1 + (lane & 3) % 3, 0)) * 64;
                if constexpr (it == 3) { if (w == 0) dma16_dev<L_FLG, 0>(ws + WS_FP, afl, L0); }
                else {
                    if (!__all(*(const LAS u64*)(L + L_FLG + lane * 16) == tag)) wait_flag(ws + WS_FP + afl, tag);
                    const char* s = w < 5 ? ws + WS_KV + ((h * 64 + max(rt * 4 + w - 1, 0)) << 12) : ws + WS_Q + ((h * 64 + rt * 4) << 11) + (w > 5) * 4096;
                    dma16_dev4<L_LKV>(s, lane16, L0 + min(w, 6) * 4096);
                }
            }
            if constexpr (it == 6) {
                if (g == 1) {
                    const lds_cptr e = (lds_cptr)L + L_LKV + j * 4096 + lane * 16;
                    const bf16x8 lq0 = ldsfrag((lds_cptr)L + L_LQ + j * 2048 + lane * 16), lq1 = ldsfrag((lds_cptr)L + L_LQ + j * 2048 + 1024 + lane * 16);
                    const bool first = (rt == 0) && (j == 0);
                    const u64 win = (0x5555ull << (r + 18)) >> (4 * hh);
                    const unsigned wc = ~(unsigned)(win >> 32), wp = first ? ~0u : ~(unsigned)win;
                    f32x16 sc, sp;
#pragma unroll
                    for (int i = 0; i < 16; ++i) {
                        const int pos = (i & 3) + 8 * (i >> 2);
                        sc[i] = __uint_as_float((unsigned)__builtin_amdgcn_sbfe(wc, pos, 1) & 0xff800000u);
                        sp[i] = i >= 8 ? __uint_as_float((unsigned)__builtin_amdgcn_sbfe(wp, pos, 1) & 0xff800000u) : 0.f;
                    }
                    sc = MFMA32(ldsfrag(e + 4096), lq0, sc); sp = MFMA32(ldsfrag(e), lq0, sp);
                    sc = MFMA32(ldsfrag(e + 5120), lq1, sc); sp = MFMA32(ldsfrag(e + 1024), lq1, sp);
                    const lds_cptr e0 = (lds_cptr)L + L_LKV + j * 4096 + vo0, e1 = (lds_cptr)L + L_LKV + j * 4096 + vo1;
                    const bf16x8 lv0 = cat8(trread(e0 + 4096), trread(e1 + 4096)), lv1 = cat8(trread(e0 + 4352), trread(e1 + 4352)), lvp = cat8(trread(e0 + 256), trread(e1 + 256));
                    const int nmask = first ? 7 - (r >> 1) : 0;
                    float lm = fmaxf(colmax16(sc), nmask > 0 ? -1e9f * LOG2E : -INFINITY);
                    lm = fmaxf(fmaxf(lm, sp[8]), sp[9]); float l2 = fmaxf(fmaxf(sp[10], sp[11]), sp[12]); lm = fmaxf(fmaxf(lm, sp[13]), sp[14]); lm = fmaxf(fmaxf(lm, l2), sp[15]);
                    lm = halves_max(lm);
#pragma unroll
                    for (int i = 0; i < 16; ++i) { sc[i] = EX2(sc[i] - lm); if (i >= 8) sp[i] = EX2(sp[i] - lm); }
                    if (first) sc[0] += (nmask > 0 && hh == 0) ? (float)nmask * EX2(-1e9f * LOG2E - lm) : 0.f;
                    f32x16 lo = {};
                    lo = MFMA32(lv0, asfrag(pack8(sc, 0)), lo); lo = MFMA32(lv1, asfrag(pack8(sc, 1)), lo); lo = MFMA32(lvp, asfrag(pack8(sp, 1)), lo);
                    const float rl = __builtin_amdgcn_rcpf(denom(lo, odd)), rlo = (odd && !hh) ? 0.f : rl, rhi = (!odd && hh) ? 0.f : rl;
#pragma unroll
                    for (int i = 0; i < 16; ++i) lo[i] *= (i < 4 ? rlo : (i >= 12 ? rhi : rl));
                    const unsigned od = WS_OA + (((rt * 4 + j) * 16 + h) << 11) + lane * 16;
                    __builtin_amdgcn_raw_buffer_store_b128(pack8(lo, 0), wsr, od, 0, 16);
                    __builtin_amdgcn_raw_buffer_store_b128(pack8(lo, 1), wsr, od + 1024, 0, 16);
                }
            }
            if constexpr (it == 7) { if (tid == 256) set_flag(ws + WS_FOA + (rt * 16 + h) * 64, tag); }
            if constexpr (it == 12) { if (w == 0) dma16_dev<L_FLG, 0>(ws + WS_FOA + rt * 1024, (lane & 15) * 64, L0); }
            if constexpr (it == 13) { if (!__all(*(const LAS u64*)(L + L_FLG + lane * 16) == tag)) wait_flag(ws + WS_FOA + rt * 1024 + (lane & 15) * 64, tag); }
        };
        ITER(0); ITER(1); ITER(2); ITER(3); ITER(4); ITER(5); ITER(6); ITER(7);
        ITER(8); ITER(9); ITER(10); ITER(11); ITER(12); ITER(13); ITER(14); ITER(15);
        pB[2] = cvtpk(et[0], et[1]); pB[3] = cvtpk(et[2], et[3]);
        o = MFMA32(vf1, asfrag(pB), o);
        STAMP(7);
        {
            f32x16 ab;
#pragma unroll
            for (int i = 0; i < 16; ++i) ab[i] = fabsf(o[i]);
            if (__builtin_expect(__any(!(colmax16(ab) < 0x1p120f)), 0)) {
                o = f32x16{}; m = -INFINITY;
                const lds_cptr e = (lds_cptr)L + L_CMB + w * 4096;
#pragma nounroll
                for (int n = 0; n < 32; ++n) {
                    asm volatile("s_waitcnt lgkmcnt(0)" ::: "memory");
                    dma16_dev4<L_CMB>(ws + WS_KV + (1u << 22) + ((h * 64 + (n >> 1) * 4 + 2 * g + (n & 1)) << 12), lane16, L0 + w * 4096);
                    WAIT_V(0);
                    f32x16 s = {};
                    s = MFMA32(ldsfrag(e + lane * 16), q0, s); s = MFMA32(ldsfrag(e + 1024 + lane * 16), q1, s);
                    const float mn = fmaxf(m, halves_max(colmax16(s))), al = EX2(m - mn);
                    m = mn;
#pragma unroll
                    for (int i = 0; i < 16; ++i) { s[i] = EX2(s[i] - mn); o[i] *= al; }
                    o = MFMA32(cat8(trread(e + vo0), trread(e + vo1)), asfrag(pack8(s, 0)), o);
                    o = MFMA32(cat8(trread(e + vo0 + 256), trread(e + vo1 + 256)), asfrag(pack8(s, 1)), o);
                }
            }
        }
        const float l = denom(o, odd);
        {
            const lds_ptr ex = L + L_CMB + w * 4096 + lane * 16;
            *(LAS f32x4*)ex = g ? (f32x4){o[0], o[1], o[2], o[3]} : (f32x4){o[8], o[9], o[10], o[11]};
            *(LAS f32x4*)(ex + 1024) = g ? (f32x4){o[4], o[5], o[6], o[7]} : (f32x4){o[12], o[13], o[14], o[15]};
            *(LAS f32x2*)(ex + 2048) = (f32x2){m, l};
        }
        WAIT_V(0);
        BAR();
        {
            const lds_cptr ex = (lds_cptr)L + L_CMB + (w ^ 4) * 4096 + lane * 16;
            const f32x2 ml = *(const LAS f32x2*)(ex + 2048);
            const f32x4 t0 = *(const LAS f32x4*)ex, t1 = *(const LAS f32x4*)(ex + 1024);
            const f32x4 u0 = g ? (f32x4){o[8], o[9], o[10], o[11]} : (f32x4){o[0], o[1], o[2], o[3]}, u1 = g ? (f32x4){o[12], o[13], o[14], o[15]} : (f32x4){o[4], o[5], o[6], o[7]};
            const float mm = fmaxf(m, ml[0]), f1 = EX2(m - mm), f2 = EX2(ml[0] - mm), rl = __builtin_amdgcn_rcpf(l * f1 + ml[1] * f2);
            const float z0 = (odd && !g && !hh) ? 0.f : rl, z1 = (!odd && g && hh) ? 0.f : rl;
            const f32x4 y0 = (u0 * f1 + t0 * f2) * z0, y1 = (u1 * f1 + t1 * f2) * z1;
            __builtin_amdgcn_raw_buffer_store_b128((u32x4){cvtpk(y0[0], y0[1]), cvtpk(y0[2], y0[3]), cvtpk(y1[0], y1[1]), cvtpk(y1[2], y1[3])}, wsr,
                                                   WS_OB + (((rt * 4 + j) * 16 + h) << 11) + g * 1024 + lane * 16, 0, 16);
        }
        WO_READ(14, 0); WO_READ(14, 1); WO_READ(15, 0); WO_READ(15, 1);
        STAMP(11);
    }
    auto publish = [&]() {
        WAIT_V(0);
        unsigned old = 0;
        if (lane == 0) old = __hip_atomic_fetch_add((LAS unsigned*)(L + L_CNT), 1u, __ATOMIC_RELAXED, __HIP_MEMORY_SCOPE_WORKGROUP);
        if (lane == 0 && old == 7) set_flag(ws + WS_FOB + (rt * 16 + h) * 64, tag);
    };
    publish();
    STAMP(8);
    if (g == 1) {
        wait_flag(ws + WS_FOB + (rt * 16 + 4 * j + (lane & 3)) * 64, tag);
        STAMP(15);
        const char* s = ws + WS_OB + (tto << 15) + j * 8192;
        dma16_dev4<L_OB>(s, lane * 16, L0 + j * 8192); dma16_dev4<L_OB + 4096>(s + 4096, lane * 16, L0 + j * 8192);
        WAIT_V(0);
        BAR_RAW();
        STAMP(9);
    }
    f32x16 acc = {};
    {
        const lds_cptr op = (lds_cptr)L + (g ? L_OB : L_OA) + lane * 16;
        bf16x8 of[8];
#pragma unroll
        for (int i = 0; i < 8; ++i) of[i] = ldsfrag(op + i * 1024);
#pragma unroll
        for (int i = 0; i < 32; ++i) { SBAR(); acc = MFMA32(wf[i], of[i & 7], acc); SBAR(); if (i < 24) of[i & 7] = ldsfrag(op + (i + 8) * 1024); }
    }
    if (cg * 4 + j < 14) {
        char* od = out + (size_t)(tto * 32 + r) * 1792 + g * 896 + (cg * 4 + j) * 64 + hh * 16;
#pragma unroll
        for (int k = 0; k < 2; ++k) {
            auto r0 = __builtin_amdgcn_permlane32_swap(cvtpk(acc[8 * k], acc[8 * k + 1]), cvtpk(acc[8 * k + 4], acc[8 * k + 5]), false, false);
            auto r1 = __builtin_amdgcn_permlane32_swap(cvtpk(acc[8 * k + 2], acc[8 * k + 3]), cvtpk(acc[8 * k + 6], acc[8 * k + 7]), false, false);
            *(u32x4*)(od + k * 32) = (u32x4){r0[0], r1[0], r0[1], r1[1]};
        }
    }
    if (b == 0 && w == 0) {
        const char* f = ws + WS_FOA + lane * 64;
        wait_flag3(f, f + 4096, f + 8192, tag); wait_flag(f + 12288, tag);
        if (lane == 0) set_flag(ws, tag);
    }
    STAMP(10);
    STAMP_FLUSH();
}

extern "C" void kernel_launch(void* const* d_in, const int* in_sizes, int n_in, void* d_out, int out_size, void* d_ws, size_t ws_size, hipStream_t stream) {
    fused_kernel<<<256, 512, 0, stream>>>((const char*)d_in[0], (const char*)d_in[1], (const char*)d_in[2], (const char*)d_in[3], (const char*)d_in[4], (const char*)d_in[5],
                                          (char*)d_ws, (char*)d_out);
}
```

```cpp
#include <hip/hip_runtime.h>
#include <hip/hip_bf16.h>
#include <stdint.h>
#include <type_traits>

#define STAMP(i)
#define STAMP_DECL
#define STAMP_FLUSH()

typedef short bf16x8 __attribute__((ext_vector_type(8)));
typedef short s16x4 __attribute__((ext_vector_type(4)));
typedef float f32x16 __attribute__((ext_vector_type(16)));
typedef float f32x4 __attribute__((ext_vector_type(4)));
typedef float f32x2 __attribute__((ext_vector_type(2)));
typedef unsigned u32x4 __attribute__((ext_vector_type(4)));
typedef unsigned u32x2 __attribute__((ext_vector_type(2)));
typedef __bf16 bf16x2_t __attribute__((ext_vector_type(2)));
typedef unsigned long long u64;
#define LAS __attribute__((address_space(3)))
#define GAS __attribute__((address_space(1)))
typedef LAS const char* lds_cptr;
typedef LAS char* lds_ptr;

constexpr unsigned WS_FP = 1u << 16;
constexpr unsigned WS_FOA = 2u << 16, WS_FOB = 4u << 16;
constexpr unsigned WS_OA = 1u << 20, WS_OB = 3u << 20;
constexpr unsigned WS_IMG = 8u << 20, IMG_T = 6144, IMG_C = 4 * IMG_T, IMG_H = 64 * IMG_T, IMG_B = 16 * IMG_H;
constexpr unsigned WS_BYTES = 24u << 20;
constexpr int P1_SLOT = 32768, P1_W = 8192;
constexpr int L_WOS = 0, L_FLG = 49152, L_RDY = 51200, L_KV = 65536, L_CMB = 65536, L_OB = 98304;
constexpr int L_OA = 131072, L_QEX = 131072, L_LKV = 131072, LDS_BYTES = 163840;
#define FLAG_STEP 10
#define A_PLAIN 0
#define PF_LIMIT 300
#define PF_WAIT 600
#define KV_ROT 0
#define LOCAL_GROUP 0
#define WAVE_FLAGS 0
#define PF_SLEEP 0
constexpr float LOG2E = 1.4426950408889634f;
constexpr float QSCALE = 0.18898223650461362f * LOG2E;
constexpr float M_BIAS = 40.f;

#define MFMA32(a, b, c) __builtin_amdgcn_mfma_f32_32x32x16_bf16(a, b, c, 0, 0, 0)
#define WAIT_V(n) asm volatile("s_waitcnt vmcnt(%0)" :: "i"(n) : "memory")
#define BAR() asm volatile("s_waitcnt lgkmcnt(0)\n\ts_barrier" ::: "memory")
#define BAR_RAW() asm volatile("s_barrier" ::: "memory")
#define SBAR() __builtin_amdgcn_sched_barrier(0)
#define PIN(x) asm volatile("" : "+v"(x))
#define RLX_AGENT __ATOMIC_RELAXED, __HIP_MEMORY_SCOPE_AGENT
#define EX2(x) __builtin_amdgcn_exp2f(x)

__device__ __forceinline__ unsigned cvtpk(float lo, float hi) { return __builtin_bit_cast(unsigned, __builtin_convertvector((f32x2){lo, hi}, bf16x2_t)); }
template <int OFF> __device__ __forceinline__ void dma16(const char* base, unsigned voff, unsigned lds) {
    asm volatile("s_add_u32 m0, %2, %3\n\ts_nop 0\n\tglobal_load_lds_dwordx4 %0, %1" :: "v"(voff), "s"(base), "s"(lds), "i"(OFF) : "memory", "scc"); }
template <int OFF, int IO> __device__ __forceinline__ void dma16_dev(const char* base, unsigned voff, unsigned lds) {
    asm volatile("s_add_u32 m0, %2, %3\n\ts_nop 0\n\tglobal_load_lds_dwordx4 %0, %1 offset:%4 sc1" :: "v"(voff), "s"(base), "s"(lds), "i"(OFF), "i"(IO) : "memory", "scc"); }
template <int OFF> __device__ __forceinline__ void dma16_dev2(const char* base, unsigned voff, unsigned lds) {
    asm volatile("s_add_u32 m0, %2, %3\n\ts_nop 0\n\tglobal_load_lds_dwordx4 %0, %1 sc1\n\tglobal_load_lds_dwordx4 %0, %1 offset:1024 sc1" :: "v"(voff), "s"(base), "s"(lds), "i"(OFF) : "memory", "scc"); }
template <int OFF> __device__ __forceinline__ void dma16_dev4(const char* base, unsigned voff, unsigned lds) {
    asm volatile("s_add_u32 m0, %2, %3\n\ts_nop 0\n\tglobal_load_lds_dwordx4 %0, %1 sc1\n\tglobal_load_lds_dwordx4 %0, %1 offset:1024 sc1\n\t"
                 "global_load_lds_dwordx4 %0, %1 offset:2048 sc1\n\tglobal_load_lds_dwordx4 %0, %1 offset:3072 sc1" :: "v"(voff), "s"(base), "s"(lds), "i"(OFF) : "memory", "scc"); }
template <int N> __device__ __forceinline__ void dma16_x8(const char* base, const unsigned (&v)[8], unsigned lds, unsigned ls) {
    asm volatile("s_mov_b32 m0, %9\n\ts_waitcnt vmcnt(%11)\n\tglobal_load_lds_dwordx4 %0, %8\n\t"
                 "s_add_u32 m0, m0, %10\n\ts_waitcnt vmcnt(%11)\n\tglobal_load_lds_dwordx4 %1, %8\n\t" "s_add_u32 m0, m0, %10\n\ts_waitcnt vmcnt(%11)\n\tglobal_load_lds_dwordx4 %2, %8\n\t"
                 "s_add_u32 m0, m0, %10\n\ts_waitcnt vmcnt(%11)\n\tglobal_load_lds_dwordx4 %3, %8\n\t" "s_add_u32 m0, m0, %10\n\ts_waitcnt vmcnt(%11)\n\tglobal_load_lds_dwordx4 %4, %8\n\t"
                 "s_add_u32 m0, m0, %10\n\ts_waitcnt vmcnt(%11)\n\tglobal_load_lds_dwordx4 %5, %8\n\t" "s_add_u32 m0, m0, %10\n\ts_waitcnt vmcnt(%11)\n\tglobal_load_lds_dwordx4 %6, %8\n\t"
                 "s_add_u32 m0, m0, %10\n\ts_waitcnt vmcnt(%11)\n\tglobal_load_lds_dwordx4 %7, %8"
                 :: "v"(v[0]), "v"(v[1]), "v"(v[2]), "v"(v[3]), "v"(v[4]), "v"(v[5]), "v"(v[6]), "v"(v[7]), "s"(base), "s"(lds), "s"(ls), "i"(N) : "memory", "scc"); }
__device__ __forceinline__ s16x4 trread(lds_cptr p) { return __builtin_bit_cast(s16x4, __builtin_amdgcn_ds_read_tr16_b64_v4i16((LAS s16x4*)p)); }
__device__ __forceinline__ bf16x8 ldsfrag(lds_cptr p) { return *(const LAS bf16x8*)p; }
__device__ __forceinline__ bf16x8 cat8(s16x4 a, s16x4 b) { return (bf16x8){a[0], a[1], a[2], a[3], b[0], b[1], b[2], b[3]}; }
__device__ __forceinline__ u32x4 pack8(const f32x16& a, int s) {
    return (u32x4){cvtpk(a[8 * s], a[8 * s + 1]), cvtpk(a[8 * s + 2], a[8 * s + 3]), cvtpk(a[8 * s + 4], a[8 * s + 5]), cvtpk(a[8 * s + 6], a[8 * s + 7])};
}
__device__ __forceinline__ bf16x8 asfrag(u32x4 v) { return __builtin_bit_cast(bf16x8, v); }
__device__ __forceinline__ float max3f(float a, float b, float c) { float d; asm("v_max3_f32 %0, %1, %2, %3" : "=v"(d) : "v"(a), "v"(b), "v"(c)); return d; }
__device__ __forceinline__ float halves_max(float x) {
    auto r = __builtin_amdgcn_permlane32_swap(__float_as_uint(x), __float_as_uint(x), false, false);
    return fmaxf(__uint_as_float(r[0]), __uint_as_float(r[1]));
}
__device__ __forceinline__ float colmax16(const f32x16& s) {
    float a = fmaxf(fmaxf(s[0], s[1]), s[2]), b = fmaxf(fmaxf(s[3], s[4]), s[5]);
    a = fmaxf(fmaxf(a, s[6]), s[7]); b = fmaxf(fmaxf(b, s[8]), s[9]);
    a = fmaxf(fmaxf(a, s[10]), s[11]); b = fmaxf(fmaxf(b, s[12]), s[13]);
    a = fmaxf(fmaxf(a, s[14]), s[15]);
    return fmaxf(a, b);
}
__device__ __forceinline__ float denom(const f32x16& o, bool odd) {
    const float own = odd ? o[0] : o[12];
    auto r = __builtin_amdgcn_permlane32_swap(__float_as_uint(own), __float_as_uint(own), false, false);
    return __uint_as_float(odd ? r[0] : r[1]);
}
__device__ __forceinline__ void wait_flag(const char* f, u64 tag) {
#pragma nounroll
    for (unsigned s = 0; s < (1u << 22); ++s) {
        if (__all(__hip_atomic_load((GAS u64*)f, RLX_AGENT) == tag)) break;
        __builtin_amdgcn_s_sleep(2);
    }
    asm volatile("" ::: "memory");
}
__device__ __forceinline__ void wait_flag3(const char* f0, const char* f1, const char* f2, u64 tag) {
#pragma nounroll
    for (unsigned s = 0; s < (1u << 22); ++s) {
        const u64 a = __hip_atomic_load((GAS u64*)f0, RLX_AGENT), b = __hip_atomic_load((GAS u64*)f1, RLX_AGENT), c = __hip_atomic_load((GAS u64*)f2, RLX_AGENT);
        if (__all(a == tag && b == tag && c == tag)) break;
        __builtin_amdgcn_s_sleep(4);
    }
    asm volatile("" ::: "memory");
}
__device__ __forceinline__ void set_flag(char* f, u64 tag) { __hip_atomic_store((GAS u64*)f, tag, RLX_AGENT); }

__device__ __forceinline__ unsigned tile_addr(unsigned G, unsigned base, unsigned r) {
    const unsigned P = __umul24(G, 293) >> 11, gi = G - __umul24(P, 7), hi = gi >> 2, p = gi - __umul24(hi, 3);
    return base + __umul24(2 * (P & 7) + hi, IMG_H) + ((P >> 3) << 11) + (p << 9) + ((r ^ (__umul24(P >> 4, p) << 2)) << 4);
}
__device__ __forceinline__ u32x4 pack_half1(const f32x16& a, int k, float sc) {
    float t0 = a[8 * k] * sc, t2 = a[8 * k + 2] * sc, t4 = a[8 * k + 4] * sc, t6 = a[8 * k + 6] * sc; PIN(t0); PIN(t2); PIN(t4); PIN(t6);
    u32x4 v = (u32x4){cvtpk(t0, a[8 * k + 1] * sc), cvtpk(t2, a[8 * k + 3] * sc), cvtpk(t4, a[8 * k + 5] * sc), cvtpk(t6, a[8 * k + 7] * sc)};
    PIN(v);
    return v;
}
__device__ __forceinline__ void pack_half2(u32x4& v, bool split, u32x4& e) {
    auto r0 = __builtin_amdgcn_permlane32_swap(v[0], v[2], false, false);
    auto r1 = __builtin_amdgcn_permlane32_swap(v[1], v[3], false, false);
    e[2] = split ? r0[1] : e[2]; e[3] = split ? r1[1] : e[3];
    v = (u32x4){r0[0], r1[0], split ? e[0] : r0[1], split ? 0u : r1[1]};
    PIN(v); PIN(e);
}

__global__ __launch_bounds__(512, 2) void fused_kernel(const char* __restrict__ xA, const char* __restrict__ wA, const char* __restrict__ woA,
                                                       const char* __restrict__ xB, const char* __restrict__ wB, const char* __restrict__ woB,
                                                       char* ws, char* __restrict__ out) {
    __shared__ __attribute__((aligned(16))) char smem[LDS_BYTES];
    asm volatile("" :: "s"(xA), "s"(wA), "s"(woA), "s"(xB), "s"(wB), "s"(woB), "s"(ws), "s"(out));
    const lds_ptr L = (lds_ptr)smem;
    const unsigned L0 = (unsigned)(uintptr_t)L;
    const int tid = threadIdx.x, lane = tid & 63, w = __builtin_amdgcn_readfirstlane(tid >> 6);
    const int g = w >> 2, j = w & 3, r = lane & 31, hh = lane >> 5;
    const int b = blockIdx.x, xcd = b & 7, loc = b >> 3;
    const int rt = loc & 15, h = 2 * xcd + (loc >> 4);
    const int prl = loc < 24 ? (loc & 7) : (loc & 3) + 4 * (xcd & 1), pcl = loc >> 3;
    const int pr = (xcd >> 1) * 8 + prl, pc = loc < 24 ? pcl + 4 * (xcd & 1) : 3;
    STAMP_DECL;
    STAMP(0);
    const __amdgpu_buffer_rsrc_t wsr = __builtin_amdgcn_make_buffer_rsrc(ws, 0, WS_BYTES, 0x00020000);
    u64 tag;
#define LOAD_TAG() do { asm volatile("s_load_dwordx2 %0, %1, 0x0\n\ts_waitcnt lgkmcnt(0)" : "=s"(tag) : "s"(ws) : "memory"); tag += 1; } while (0)
    const int fcol = 28 * h + 27 * hh;
    const char* imgB = ws + WS_IMG + IMG_B + h * IMG_H;
    const char* imgA = ws + WS_IMG + h * IMG_H;
    const int rot = KV_ROT ? rt : 0;
    const char* kvf0;
    auto kv_flags = [&]() {
        int ln = lane; asm volatile("" : "+v"(ln));
        kvf0 = ws + WS_FP + 16384 + ((((ln >> 2) & 3) % 3 * 448 + 28 * h + 27 * (ln >> 5)) / 192 * 32 + (((ln >> 2) & 3) % 3 == 0 ? rt : ((rot + (ln & 3)) & 15)) * 2 + (j >> 1)) * 64;
    };
    auto kv_prefetch = [&](bool ok) {
        if (!ok) wait_flag(kvf0, tag);
        STAMP(3);
        const char* s = imgB + j * IMG_T + 2048;
        dma16_dev2<L_QEX>(imgB + (rt * 4 + j) * IMG_T, lane * 16, L0 + j * 2048);
        dma16_dev4<L_KV>(s + rot * IMG_C, lane * 16, L0 + j * 4096);
        dma16_dev4<L_KV + 16384>(s + ((rot + 1) & 15) * IMG_C, lane * 16, L0 + j * 4096);
        dma16_dev4<L_KV + 32768>(s + ((rot + 2) & 15) * IMG_C, lane * 16, L0 + j * 4096);
    };

    unsigned sad[7];
    u32x4 pv[7];
    if (__builtin_expect(loc < 28, 1)) {
        const int ct0 = pc * 6 + 3 * (j >> 1);
        if (__builtin_expect(g == 1, 1)) {
            unsigned vo[8];
            {
                const unsigned p = 64 * (j - 1) + lane, prow = (p * 2731) >> 16, pcn = p - 24 * prow;
                const unsigned st2 = j == 0 ? 16 * 896 : 16 * 2688;
                vo[0] = j == 0 ? (lane >> 3) * 896 + ((lane & 7) ^ (lane >> 4)) * 16 : prow * 2688 + (pcn ^ ((prow & 2) << 1)) * 16;
                vo[1] = j == 0 ? (vo[0] ^ 64) + 8 * 896 : vo[0] + 8 * 2688;
#pragma unroll
                for (int i = 2; i < 8; ++i) vo[i] = vo[i - 2] + st2;
            }
            const int start = (prl + 2 * pcl) % 7;
            const int roleoff = j == 0 ? pr * (64 * 896) : pc * 384;
            const int kstride = j == 0 ? 128 : 64 * 2688;
            const unsigned lrole = L0 + (j == 0 ? 0 : P1_W + (j - 1) * 1024), lstep = j == 0 ? 1024 : 3072;
            const char* src = (j == 0 ? xB : wB) + roleoff;
            int ikc = start;
#define P1_ISSUE(c) do { dma16_x8<40>(src + ikc * kstride, vo, lrole + ((c) & 3) * P1_SLOT, lstep); ikc = ikc == 6 ? 0 : ikc + 1; } while (0)
#define P1_FEED(c, n) do { WAIT_V(n); STAMP(16 + (c)); BAR_RAW(); P1_ISSUE((c) + 2); STAMP(48 + (c)); } while (0)
            P1_ISSUE(0); P1_ISSUE(1);
            STAMP(1);
            LOAD_TAG();
            P1_FEED(0, 8);
            kv_flags(); PIN(kvf0);
#pragma nounroll
            for (int c = 1; c < 12; ++c) {
                if (c == 5) { src = (j == 0 ? xA : wA) + roleoff; if (A_PLAIN) ikc = 0; }
                WAIT_V(8); BAR_RAW(); P1_ISSUE(c + 2);
            }
            WAIT_V(8); BAR_RAW();
            u64 f0;
            asm volatile("global_load_dwordx2 %0, %1, off sc1\n\ts_waitcnt vmcnt(1)\n\ts_barrier\n\ts_waitcnt vmcnt(0)" : "=&v"(f0) : "v"(kvf0) : "memory");
            STAMP(29);
            kv_prefetch(__all(f0 == tag));
        } else {
            const int xrow = 32 * (j & 1) + r;
            const int xb = xrow * 128 + ((hh ^ ((xrow >> 1) & 7)) << 4);
            const int tq = (lane & 15) >> 2;
            const int wrow = P1_W + (8 * hh + tq) * 384 + ((lane >> 4) & 1) * 32 + 8 * (lane & 3);
            const int wb0 = wrow + (((3 * (j >> 1)) ^ (tq >> 1)) << 6), wb1 = wrow + (((3 * (j >> 1) + 1) ^ (tq >> 1)) << 6), wb2 = wrow + (((3 * (j >> 1) + 2) ^ (tq >> 1)) << 6);
            LOAD_TAG();
#define P1_STEP(A0, A1, A2, C0, C1, C2, km, kr, F0, F1, F2) do { \
                A0 = MFMA32(w0[km], xf[km], C0); SBAR(); \
                xf[kr] = ldsfrag(sl + (xb ^ ((kr) << 5))); w0[kr] = cat8(trread(sl + wb0 + (kr) * 6144), trread(sl + wb0 + (kr) * 6144 + 1536)); F0; SBAR(); \
                A1 = MFMA32(w1[km], xf[km], C1); SBAR(); \
                w1[kr] = cat8(trread(sl + wb1 + (kr) * 6144), trread(sl + wb1 + (kr) * 6144 + 1536)); F1; SBAR(); \
                A2 = MFMA32(w2[km], xf[km], C2); SBAR(); \
                w2[kr] = cat8(trread(sl + wb2 + (kr) * 6144), trread(sl + wb2 + (kr) * 6144 + 1536)); F2; SBAR(); } while (0)
#define P1_READ(kr) do { xf[kr] = ldsfrag(sl + (xb ^ ((kr) << 5))); w0[kr] = cat8(trread(sl + wb0 + (kr) * 6144), trread(sl + wb0 + (kr) * 6144 + 1536)); \
                w1[kr] = cat8(trread(sl + wb1 + (kr) * 6144), trread(sl + wb1 + (kr) * 6144 + 1536)); w2[kr] = cat8(trread(sl + wb2 + (kr) * 6144), trread(sl + wb2 + (kr) * 6144 + 1536)); SBAR(); } while (0)
#define P1_PLAIN(A0, A1, A2, km, kr) P1_STEP(A0, A1, A2, A0, A1, A2, km, kr, (void)0, (void)0, (void)0)
#define PK1(A, t, k) pv[2 * (t) + (k)] = pack_half1(A, k, sc##t)
#define PK2(i) pack_half2(pv[i], isp == (i), pv[6])
#define PUBAUX 16
#define PUB(i) __builtin_amdgcn_raw_buffer_store_b128(pv[i], wsr, sad[i], 0, PUBAUX)
#define PK2PUB(i) do { PK2(i); PUB(i); } while (0)
#define NOP_ (void)0
            const f32x16 zero = {};
            float sc0 = ct0 < 14 ? QSCALE : 1.f, sc1 = ct0 + 1 < 14 ? QSCALE : 1.f, sc2 = ct0 + 2 < 14 ? QSCALE : 1.f;
            PIN(sc0); PIN(sc1); PIN(sc2);
            unsigned isp;
            {
                const unsigned sbase = WS_IMG + IMG_B + (pr * 2 + (j & 1)) * IMG_T, Gb = ct0 * 4;
#pragma unroll
                for (int i = 0; i < 6; ++i) { sad[i] = tile_addr(Gb + 2 * i + hh, sbase, r); PIN(sad[i]); }
                const unsigned rem = Gb % 7, g0 = rem <= 3 ? 3 - rem : 10 - rem, gl = g0 + (((g0 ^ hh) & 1) ? 7 : 0);
                isp = gl >> 1;
                sad[6] = gl < 12 ? (tile_addr(Gb + gl, sbase, r) ^ (Gb + gl >= 112 ? 192 : 0)) + (IMG_H - 1536) : 0x80000000u;
                pv[6] = (u32x4){Gb + gl >= 112 ? 0x3f80u : 0u, 0u, 0u, 0u};
                PIN(isp); PIN(sad[6]); PIN(pv[6]);
            }
            f32x16 a0, a1, a2, b0, b1, b2;
            bf16x8 xf[4], w0[4], w1[4], w2[4];
            {
                BAR_RAW(); STAMP(16);
                const lds_cptr sl = (lds_cptr)L;
                P1_READ(0); P1_READ(1); P1_STEP(a0, a1, a2, zero, zero, zero, 0, 2, NOP_, NOP_, NOP_); P1_PLAIN(a0, a1, a2, 1, 3);
            }
#pragma nounroll
            for (int c = 1; c < 7; ++c) {
                BAR_RAW(); STAMP(16 + c);
                const lds_cptr sl = (lds_cptr)L + (c & 3) * P1_SLOT;
                P1_PLAIN(a0, a1, a2, 2, 0); P1_PLAIN(a0, a1, a2, 3, 1); P1_PLAIN(a0, a1, a2, 0, 2); P1_PLAIN(a0, a1, a2, 1, 3);
            }
            {
                BAR_RAW(); STAMP(23);
                int so = 3 * P1_SLOT; asm volatile("" : "+s"(so));
                const lds_cptr sl = (lds_cptr)L + so;
                P1_PLAIN(a0, a1, a2, 2, 0); P1_PLAIN(a0, a1, a2, 3, 1);
                P1_STEP(b0, b1, b2, zero, zero, zero, 0, 2, PK1(a0, 0, 0), PK2PUB(0), PK1(a0, 0, 1));
                P1_STEP(b0, b1, b2, b0, b1, b2, 1, 3, PK2PUB(1), PK1(a1, 1, 0), PK2PUB(2));
            }
            {
                BAR_RAW(); STAMP(24);
                const lds_cptr sl = (lds_cptr)L;
                P1_STEP(b0, b1, b2, b0, b1, b2, 2, 0, PK1(a1, 1, 1), PK2PUB(3), NOP_);
                P1_STEP(b0, b1, b2, b0, b1, b2, 3, 1, PK1(a2, 2, 0), PK2PUB(4), NOP_);
                P1_STEP(b0, b1, b2, b0, b1, b2, 0, 2, PK1(a2, 2, 1), PK2PUB(5), NOP_);
                P1_STEP(b0, b1, b2, b0, b1, b2, 1, 3, PUB(6), NOP_, NOP_);
                STAMP(12);
            }
#pragma nounroll
            for (int c = 9; c < 14; ++c) {
                if (c == FLAG_STEP) WAIT_V(0);
                BAR_RAW(); STAMP(16 + c);
                if (c == FLAG_STEP && tid == 0) set_flag(ws + WS_FP + 16384 + (pc * 32 + pr) * 64, tag);
                const lds_cptr sl = (lds_cptr)L + (c & 3) * P1_SLOT;
                P1_PLAIN(b0, b1, b2, 2, 0); P1_PLAIN(b0, b1, b2, 3, 1); P1_PLAIN(b0, b1, b2, 0, 2); P1_PLAIN(b0, b1, b2, 1, 3);
            }
            b0 = MFMA32(w0[2], xf[2], b0); b0 = MFMA32(w0[3], xf[3], b0); b1 = MFMA32(w1[2], xf[2], b1); b1 = MFMA32(w1[3], xf[3], b1); SBAR();
            b2 = MFMA32(w2[2], xf[2], b2); SBAR(); PK1(b0, 0, 0); SBAR(); b2 = MFMA32(w2[3], xf[3], b2); SBAR();
            PK1(b0, 0, 1); PK1(b1, 1, 0); PK1(b1, 1, 1); PK1(b2, 2, 0); PK1(b2, 2, 1);
            PK2(0); PK2(1); PK2(2); PK2(3); PK2(4); PK2(5);
#pragma unroll
            for (int i = 0; i < 7; ++i) { sad[i] -= IMG_B; PIN(sad[i]); }
            STAMP(13);
        }
    } else {
        LOAD_TAG();
#pragma nounroll
        for (int i = 0; i < PF_SLEEP; ++i) __builtin_amdgcn_s_sleep(16);
        const int u = (loc - 28) * 8 + w;
        {
            const char* sw = wA + (xcd & 1) * 1152;
            const char* sx = xA + (xcd >> 1) * (512 * 896) + u * 7168;
            const u64 t0 = __builtin_amdgcn_s_memrealtime();
#pragma nounroll
            for (int q = 0; q < 4; ++q) {
                if (q > 0 && __builtin_amdgcn_s_memrealtime() - t0 > PF_LIMIT) break;
                const char* swq = sw + (((xcd >> 1) + q) & 3) * (112 * 2688);
#pragma nounroll
                for (int i = u; i < 168; i += 32) { const unsigned p = 64 * i + lane, row = (p * 43691) >> 22; dma16<0>(swq, row * 2688 + (p - 96 * row) * 16, L0 + w * 1024); }
                if (q < 2) {
                    const char* sxq = sx + ((xcd ^ q) & 1) * (256 * 896);
#pragma nounroll
                    for (int i = 0; i < 7; ++i) dma16<0>(sxq + i * 1024, lane * 16, L0 + w * 1024);
                }
                WAIT_V(4);
            }
            const char* so = (u & 1 ? woB : woA) + ((u >> 1) * 8 + xcd) * 3136;
#pragma nounroll
            for (int i = 0; i < 3; ++i) dma16<0>(so + i * 1024, lane * 16, L0 + w * 1024);
            while (__builtin_amdgcn_s_memrealtime() - t0 < PF_WAIT) __builtin_amdgcn_s_sleep(4);
        }
        WAIT_V(0);
        STAMP(2);
        if (g == 1) { kv_flags(); kv_prefetch(false); }
    }
    STAMP(5);
    const int tto = xcd * 8 + (loc >> 2), cg = loc & 3, rto = tto >> 2;
    const bool odd = h & 1, pub = g == 0 && loc < 28;
    bf16x8 wf[32];
    {
        f32x16 o = {};
        float m;
        bf16x8 q0, q1;
        const lds_cptr kvp = (lds_cptr)L + L_KV + g * 8192 + lane * 16;
        const unsigned wso = (8 * j + (lane >> 4)) * 896 + min((unsigned)(cg * 256 + (((lane & 15) ^ ((lane >> 4) << 2)) << 4)), 880u), wso1 = wso + 4 * 896;
        const unsigned lw2 = L0 + w * 2048, lane16 = lane * 16;
        const char* wsrc = g ? woB : woA;
        const char* kvsrc = imgB + (w >> 1) * IMG_T + 2048 + (w & 1) * 2048;
        const int tq = (lane & 15) >> 2, tp = lane & 3;
        const lds_cptr wrp = (lds_cptr)L + L_WOS + g * 8192 + (4 * hh + tq) * 256 + (((4 * j + 2 * ((lane >> 4) & 1) + (tp >> 1)) ^ (tq << 2)) << 4) + 8 * (tp & 1);
        const int vpc = 2 * ((lane >> 4) & 1) + (tp >> 1);
        const int vo0 = 2048 + vpc * 512 + (((4 * hh + tq) ^ (vpc << 2)) << 4) + 8 * (tp & 1), vo1 = vo0 ^ 128;
        const lds_cptr vp0 = (lds_cptr)L + L_KV + g * 8192 + vo0, vp1 = (lds_cptr)L + L_KV + g * 8192 + vo1;
        f32x16 sA, sB, negm;
        bf16x8 kf0, kf1, vf0, vf1;
        u32x4 pA, pB;
        f32x4 et;
#define TILE_OFF(n) ((((n) >> 1) & 3) * 16384 + ((n) & 1) * 4096)
#define EXP4(S, i) do { S[i] = EX2(S[i]); S[i + 1] = EX2(S[i + 1]); S[i + 2] = EX2(S[i + 2]); S[i + 3] = EX2(S[i + 3]); } while (0)
#define STEP(n, SC, SN, X1, X2, X3) do { \
            if ((n) < 31) SN = MFMA32(kf0, q0, negm); \
            X1; SBAR(); \
            if ((n) > 0) { pB[2] = cvtpk(et[0], et[1]); pB[3] = cvtpk(et[2], et[3]); PIN(pB); SBAR(); } \
            EXP4(SC, 0); PIN(SC); SBAR(); \
            if ((n) > 0) o = MFMA32(vf1, asfrag(pB), o); \
            vf1 = cat8(trread(vp0 + TILE_OFF(n) + 256), trread(vp1 + TILE_OFF(n) + 256)); X2; SBAR(); \
            pA[0] = cvtpk(SC[0], SC[1]); pA[1] = cvtpk(SC[2], SC[3]); PIN(pA); SBAR(); \
            EXP4(SC, 4); PIN(SC); SBAR(); \
            if ((n) < 31) SN = MFMA32(kf1, q1, SN); \
            if ((n) < 30) { kf0 = ldsfrag(kvp + TILE_OFF((n) + 2)); kf1 = ldsfrag(kvp + TILE_OFF((n) + 2) + 1024); } \
            SBAR(); \
            pA[2] = cvtpk(SC[4], SC[5]); pA[3] = cvtpk(SC[6], SC[7]); PIN(pA); SBAR(); \
            EXP4(SC, 8); PIN(SC); SBAR(); \
            o = MFMA32(vf0, asfrag(pA), o); \
            if ((n) < 31) vf0 = cat8(trread(vp0 + TILE_OFF((n) + 1)), trread(vp1 + TILE_OFF((n) + 1))); \
            X3; SBAR(); \
            pB[0] = cvtpk(SC[8], SC[9]); pB[1] = cvtpk(SC[10], SC[11]); PIN(pB); SBAR(); \
            et[0] = EX2(SC[12]); et[1] = EX2(SC[13]); et[2] = EX2(SC[14]); et[3] = EX2(SC[15]); PIN(et); SBAR(); } while (0)
#define WO_READ(p, s) do { if ((p) >= 0) wf[2 * (p) + (s)] = cat8(trread(wrp + ((p) % 3) * 16384 + (s) * 4096), trread(wrp + ((p) % 3) * 16384 + (s) * 4096 + 2048)); } while (0)
#define KV_DMA(it, u) do { if ((it) < 13) dma16_dev<L_KV + (((it) + 3) & 3) * 16384, (u) * 1024>(kvsrc + ((rot + (it) + 3) & 15) * IMG_C, lane16, lw2); \
            else if ((it) >= 14) dma16_dev<L_OA + ((it) - 14) * 16384, (u) * 1024>(ws + WS_OA + (tto << 15) + ((it) - 14) * 16384 + w * 2048, lane16, lw2); } while (0)
#define WO_DMA(it, u) dma16<L_WOS + ((it) % 3) * 16384 + (u) * 1024>(wsrc + (28 * (it) - 4 * ((it) & 1)) * 896, (u) ? wso1 : wso, lw2)
#define ITER(it) do { \
              \
            if ((it) == 0 || (it) == LA + 1) WAIT_V(8); else if (((it) == 1 || (it) == 2) && pub) WAIT_V((it) == 1 ? 8 : 7); else if ((it) == 14) WAIT_V(2); else WAIT_V(4); \
            if ((it) == 0) BAR(); else asm volatile("s_waitcnt lgkmcnt(4)\n\ts_barrier" ::: "memory"); \
            STAMP(32 + (it)); \
            side(std::integral_constant<int, 2 * (it)>{}); SBAR(); \
            STEP(2 * (it), sA, sB, WO_READ((it) - 2, 0), KV_DMA(it, 0), WO_DMA(it, 0)); \
            side(std::integral_constant<int, 2 * (it) + 1>{}); SBAR(); \
            STEP(2 * (it) + 1, sB, sA, WO_READ((it) - 2, 1), KV_DMA(it, 1), WO_DMA(it, 1)); } while (0)
        constexpr int LG = LOCAL_GROUP, LA = 6;
        f32x16 sc, sp;
        u32x4 lp0, lp1;
        float lm;
        const bool first = (rt == 0) && (j == 0);
        auto side = [&](auto nc) {
            constexpr int n = decltype(nc)::value;
            if constexpr (n == 0) {
                q0 = ldsfrag((lds_cptr)L + L_QEX + j * 2048 + lane * 16); q1 = ldsfrag((lds_cptr)L + L_QEX + j * 2048 + 1024 + lane * 16);
                sA = f32x16{};
                sA = MFMA32(ldsfrag(kvp), q0, sA); sA = MFMA32(ldsfrag(kvp + 1024), q1, sA);
                kf0 = ldsfrag(kvp + TILE_OFF(1)); kf1 = ldsfrag(kvp + TILE_OFF(1) + 1024); vf0 = cat8(trread(vp0), trread(vp1));
                if (tid < 8) *(LAS unsigned*)(L + L_RDY + tid * 4) = 0u;
                m = halves_max(colmax16(sA)) + M_BIAS;
#pragma unroll
                for (int i = 0; i < 16; ++i) { negm[i] = -m; sA[i] -= m; }
                PIN(negm); PIN(sA);
                WAIT_V(4); BAR_RAW();
                if (w >= 6) dma16_dev<L_FLG, 0>(ws + WS_FP + 16384, r * 64 + (((w - 5) * 448 + fcol) / 192) * 2048, L0 + (w - 6) * 1024);
            }
            if constexpr (n < 4) {
                if (pub) { __builtin_amdgcn_raw_buffer_store_b128(pv[2 * n], wsr, sad[2 * n], 0, 16); if constexpr (n < 3) __builtin_amdgcn_raw_buffer_store_b128(pv[2 * n + 1], wsr, sad[2 * n + 1], 0, 16); }
            }
            if constexpr (n == 2) {
                if (!__all(*(const LAS u64*)(L + L_FLG + lane * 16) == tag && *(const LAS u64*)(L + L_FLG + 1024 + lane * 16) == tag)) {
                    wait_flag(ws + WS_FP + 16384 + r * 64 + ((448 + fcol) / 192) * 2048, tag); wait_flag(ws + WS_FP + 16384 + r * 64 + ((896 + fcol) / 192) * 2048, tag);
                }
            }
            if constexpr (n == 6) { if (tid == 256 && loc < 28) set_flag(ws + WS_FP + (pc * 32 + pr) * 64, tag); }
            if constexpr (n == 2 * LA - 2 || n == 2 * LA) {
                if constexpr (n == 2 * LA - 2) { if (w == 0) dma16_dev<L_FLG, 0>(ws + WS_FP, ((((lane >> 2) & 3) % 3 * 448 + fcol) / 192 * 32 + max(2 * rt - 1 + (lane & 3) % 3, 0)) * 64, L0); }
                else if (!__all(*(const LAS u64*)(L + L_FLG + lane * 16) == tag)) wait_flag(ws + WS_FP + ((((lane >> 2) & 3) % 3 * 448 + fcol) / 192 * 32 + max(2 * rt - 1 + (lane & 3) % 3, 0)) * 64, tag);
            }
            if constexpr (n == 2 * LA || n == 2 * LA + 1) {
                const int pcs = min(2 * w + (n & 1), 13);
                dma16_dev2<L_LKV>(imgA + (rt * 4 - 1) * (int)IMG_T + 2048 + pcs * 2048 + ((rt == 0 && pcs < 2) ? IMG_T : 0), lane16, L0 + pcs * 2048);
            }
            if constexpr (n == 2 * LA + 4) {
                if (g == LG) {
                    const lds_cptr e = (lds_cptr)L + L_LKV + j * IMG_T + lane * 16;
                    const bf16x8 lq0 = ldsfrag(e + 4096), lq1 = ldsfrag(e + 5120), kc0 = ldsfrag(e + 6144), kc1 = ldsfrag(e + 7168), kp0 = ldsfrag(e), kp1 = ldsfrag(e + 1024);
                    const u64 win = (0x5555ull << (r + 18)) >> (4 * hh);
                    const unsigned wc = ~(unsigned)(win >> 32), wp = first ? ~0u : ~(unsigned)win;
#pragma unroll
                    for (int i = 0; i < 16; ++i) {
                        const int pos = (i & 3) + 8 * (i >> 2);
                        sc[i] = __uint_as_float((unsigned)__builtin_amdgcn_sbfe(wc, pos, 1) & 0xff800000u);
                        sp[i] = i >= 8 ? __uint_as_float((unsigned)__builtin_amdgcn_sbfe(wp, pos, 1) & 0xff800000u) : sc[0];
                    }
                    sc = MFMA32(kc0, lq0, sc); sp = MFMA32(kp0, lq0, sp); sc = MFMA32(kc1, lq1, sc); sp = MFMA32(kp1, lq1, sp);
                }
            }
            if constexpr (n == 2 * LA + 5) {
                if (g == LG) {
                    lm = fmaxf(colmax16(sc), (first && r < 14) ? -1e9f * LOG2E : -INFINITY);
                    lm = fmaxf(fmaxf(lm, sp[8]), sp[9]); float l2 = fmaxf(fmaxf(sp[10], sp[11]), sp[12]); lm = fmaxf(fmaxf(lm, sp[13]), sp[14]); lm = fmaxf(fmaxf(lm, l2), sp[15]);
                    lm = halves_max(lm);
#pragma unroll
                    for (int i = 0; i < 8; ++i) sc[i] = EX2(sc[i] - lm);
                    if (first) sc[0] += (lane < 14) ? (float)(7 - (r >> 1)) * EX2(-1e9f * LOG2E - lm) : 0.f;
                    lp0 = pack8(sc, 0);
                }
            }
            if constexpr (n == 2 * LA + 6) {
                if (g == LG) {
#pragma unroll
                    for (int i = 8; i < 16; ++i) sc[i] = EX2(sc[i] - lm);
                    lp1 = pack8(sc, 1);
                }
            }
            if constexpr (n == 2 * LA + 7) {
                if (g == LG) {
                    const lds_cptr e0 = (lds_cptr)L + L_LKV + j * IMG_T + vo0, e1 = (lds_cptr)L + L_LKV + j * IMG_T + vo1;
                    const bf16x8 lv0 = cat8(trread(e0 + IMG_T), trread(e1 + IMG_T)), lv1 = cat8(trread(e0 + IMG_T + 256), trread(e1 + IMG_T + 256)), lvp = cat8(trread(e0 + 256), trread(e1 + 256));
#pragma unroll
                    for (int i = 8; i < 16; ++i) sp[i] = EX2(sp[i] - lm);
                    sc = f32x16{};
                    sc = MFMA32(lv0, asfrag(lp0), sc); sc = MFMA32(lv1, asfrag(lp1), sc); sc = MFMA32(lvp, asfrag(pack8(sp, 1)), sc);
                }
            }
            if constexpr (n == 2 * LA + 8) {
                if (g == LG) {
                    const float rl = __builtin_amdgcn_rcpf(denom(sc, odd)), rlo = (odd && !hh) ? 0.f : rl, rhi = (!odd && hh) ? 0.f : rl;
#pragma unroll
                    for (int i = 0; i < 16; ++i) { sc[i] *= (i < 4 ? rlo : (i >= 12 ? rhi : rl)); if (!(i & 1)) PIN(sc[i]); }
                    const unsigned od = WS_OA + (((rt * 4 + j) * 16 + h) << 11) + lane * 16;
                    __builtin_amdgcn_raw_buffer_store_b128(pack8(sc, 0), wsr, od, 0, 16);
                    __builtin_amdgcn_raw_buffer_store_b128(pack8(sc, 1), wsr, od + 1024, 0, 16);
                }
            }
            if constexpr (n == 2 * LA + 10) { if (tid == 256) set_flag(ws + WS_FOA + (rt * 16 + h) * 64, tag); }
            if constexpr (n == 26) { if (w == 0) dma16_dev<L_FLG, 0>(ws + WS_FOA + rto * 1024, (lane & 15) * 64, L0); }
            if constexpr (n == 28) { if (!__all(*(const LAS u64*)(L + L_FLG + lane * 16) == tag)) wait_flag(ws + WS_FOA + rto * 1024 + (lane & 15) * 64, tag); }
        };
        ITER(0); ITER(1); ITER(2); ITER(3); ITER(4); ITER(5); ITER(6); ITER(7);
        ITER(8); ITER(9); ITER(10); ITER(11); ITER(12); ITER(13); ITER(14); ITER(15);
        pB[2] = cvtpk(et[0], et[1]); pB[3] = cvtpk(et[2], et[3]);
        o = MFMA32(vf1, asfrag(pB), o);
        STAMP(7);
        {
            f32x16 ab;
#pragma unroll
            for (int i = 0; i < 16; ++i) ab[i] = fabsf(o[i]);
            if (__builtin_expect(__any(!(colmax16(ab) < 0x1p120f)), 0)) {
                o = f32x16{}; m = -INFINITY;
                const lds_cptr e = (lds_cptr)L + L_CMB + w * 4096;
#pragma nounroll
                for (int n = 0; n < 32; ++n) {
                    asm volatile("s_waitcnt lgkmcnt(0)" ::: "memory");
                    dma16_dev4<L_CMB>(imgB + ((n >> 1) * 4 + 2 * g + (n & 1)) * IMG_T + 2048, lane16, L0 + w * 4096);
                    WAIT_V(0);
                    f32x16 s = {};
                    s = MFMA32(ldsfrag(e + lane * 16), q0, s); s = MFMA32(ldsfrag(e + 1024 + lane * 16), q1, s);
                    const float mn = fmaxf(m, halves_max(colmax16(s))), al = EX2(m - mn);
                    m = mn;
#pragma unroll
                    for (int i = 0; i < 16; ++i) { s[i] = EX2(s[i] - mn); o[i] *= al; }
                    o = MFMA32(cat8(trread(e + vo0), trread(e + vo1)), asfrag(pack8(s, 0)), o);
                    o = MFMA32(cat8(trread(e + vo0 + 256), trread(e + vo1 + 256)), asfrag(pack8(s, 1)), o);
                }
            }
        }
        const float l = denom(o, odd);
        {
            const lds_ptr ex = L + L_CMB + w * 4096 + lane * 16;
            *(LAS f32x4*)ex = g ? (f32x4){o[0], o[1], o[2], o[3]} : (f32x4){o[8], o[9], o[10], o[11]};
            *(LAS f32x4*)(ex + 1024) = g ? (f32x4){o[4], o[5], o[6], o[7]} : (f32x4){o[12], o[13], o[14], o[15]};
            *(LAS f32x2*)(ex + 2048) = (f32x2){m, l};
        }
        BAR();
        {
            const lds_cptr ex = (lds_cptr)L + L_CMB + (w ^ 4) * 4096 + lane * 16;
            const f32x2 ml = *(const LAS f32x2*)(ex + 2048);
            const f32x4 t0 = *(const LAS f32x4*)ex, t1 = *(const LAS f32x4*)(ex + 1024);
            const f32x4 u0 = g ? (f32x4){o[8], o[9], o[10], o[11]} : (f32x4){o[0], o[1], o[2], o[3]}, u1 = g ? (f32x4){o[12], o[13], o[14], o[15]} : (f32x4){o[4], o[5], o[6], o[7]};
            const float mm = fmaxf(m, ml[0]), f1 = EX2(m - mm), f2 = EX2(ml[0] - mm), rl = __builtin_amdgcn_rcpf(l * f1 + ml[1] * f2);
            const float z0 = (odd && !g && !hh) ? 0.f : rl, z1 = (!odd && g && hh) ? 0.f : rl;
            const f32x4 y0 = (u0 * f1 + t0 * f2) * z0, y1 = (u1 * f1 + t1 * f2) * z1;
            __builtin_amdgcn_raw_buffer_store_b128((u32x4){cvtpk(y0[0], y0[1]), cvtpk(y0[2], y0[3]), cvtpk(y1[0], y1[1]), cvtpk(y1[2], y1[3])}, wsr,
                                                   WS_OB + (((rt * 4 + j) * 16 + h) << 11) + g * 1024 + lane * 16, 0, 16);
        }
        STAMP(11);
        WAIT_V(0);
        if (WAVE_FLAGS && lane == 0) set_flag(ws + WS_FOB + (((rt * 4 + j) * 16 + h) * 2 + g) * 64, tag);
        BAR_RAW();
        if (!WAVE_FLAGS && tid == 0) set_flag(ws + WS_FOB + (rt * 16 + h) * 64, tag);
        WO_READ(14, 0); WO_READ(14, 1); WO_READ(15, 0); WO_READ(15, 1);
    }
    STAMP(8);
    f32x16 acc = {};
    {
        if (g == 1) {
            wait_flag(WAVE_FLAGS ? ws + WS_FOB + ((tto * 16 + 4 * j) * 2 + (lane & 7)) * 64 : ws + WS_FOB + (rto * 16 + 4 * j + (lane & 3)) * 64, tag);
            STAMP(15);
            const char* s = ws + WS_OB + (tto << 15) + j * 8192;
            dma16_dev4<L_OB>(s, lane * 16, L0 + j * 8192); dma16_dev4<L_OB + 4096>(s + 4096, lane * 16, L0 + j * 8192);
            WAIT_V(0);
            if (lane == 0) __hip_atomic_store((LAS unsigned*)(L + L_RDY + j * 4), 1u, __ATOMIC_RELAXED, __HIP_MEMORY_SCOPE_WORKGROUP);
#pragma nounroll
            for (unsigned sp = 0; sp < (1u << 24); ++sp)
                if (__all(__hip_atomic_load((LAS unsigned*)(L + L_RDY + (lane & 3) * 4), __ATOMIC_RELAXED, __HIP_MEMORY_SCOPE_WORKGROUP) == 1u)) break;
            asm volatile("" ::: "memory");
            STAMP(9);
        }
        const lds_cptr op = (lds_cptr)L + (g ? L_OB : L_OA) + lane * 16;
        bf16x8 of[8];
#pragma unroll
        for (int i = 0; i < 8; ++i) of[i] = ldsfrag(op + i * 1024);
#pragma unroll
        for (int i = 0; i < 32; ++i) { SBAR(); acc = MFMA32(wf[i], of[i & 7], acc); SBAR(); if (i < 24) of[i & 7] = ldsfrag(op + (i + 8) * 1024); }
    }
    if (cg * 4 + j < 14) {
        char* od = out + (size_t)(tto * 32 + r) * 1792 + g * 896 + (cg * 4 + j) * 64 + hh * 16;
#pragma unroll
        for (int k = 0; k < 2; ++k) {
            auto r0 = __builtin_amdgcn_permlane32_swap(cvtpk(acc[8 * k], acc[8 * k + 1]), cvtpk(acc[8 * k + 4], acc[8 * k + 5]), false, false);
            auto r1 = __builtin_amdgcn_permlane32_swap(cvtpk(acc[8 * k + 2], acc[8 * k + 3]), cvtpk(acc[8 * k + 6], acc[8 * k + 7]), false, false);
            *(u32x4*)(od + k * 32) = (u32x4){r0[0], r1[0], r0[1], r1[1]};
        }
    }
    if (b == 0 && w == 0) {
        const char* f = ws + WS_FOA + lane * 64;
        wait_flag3(f, f + 4096, f + 8192, tag); wait_flag(f + 12288, tag);
        if (lane == 0) set_flag(ws, tag);
    }
    STAMP(10);
    STAMP_FLUSH();
}

extern "C" void kernel_launch(void* const* d_in, const int* in_sizes, int n_in, void* d_out, int out_size, void* d_ws, size_t ws_size, hipStream_t stream) {
    fused_kernel<<<256, 512, 0, stream>>>((const char*)d_in[0], (const char*)d_in[1], (const char*)d_in[2], (const char*)d_in[3], (const char*)d_in[4], (const char*)d_in[5],
                                          (char*)d_ws, (char*)d_out);
}
```
